```python
import torch

def _softmax(x, axis=-1):
    return torch.softmax(x, dim=axis)
def _gelu(x):
    return torch.nn.functional.gelu(x, approximate="tanh")
def _silu(x):
    return torch.nn.functional.silu(x)
def _sigmoid(x):
    return torch.sigmoid(x)
def _layernorm(x, axis=-1, eps=1e-5):
    m = x.mean(dim=axis, keepdim=True)
    v = x.var(dim=axis, keepdim=True, unbiased=False)
    return (x - m) / torch.sqrt(v + eps)
def _scatter_add(tgt, idx, src, axis=0):
    return tgt.clone().index_add_(axis, idx, src)
def _segment_sum(src, seg, n):
    out = torch.zeros((n,) + src.shape[1:], dtype=src.dtype)
    return out.index_add_(0, seg, src)
def _pad(x, pw):
    flat = tuple(v for pair in reversed(pw) for v in pair)
    return torch.nn.functional.pad(x, flat)

kernel_name = 'parallel_sum__grouped_kv_mqa__glu_gated_reglu__prompt'

def setup_inputs():

    _w = (lambda a: (lambda t: t.to(torch.bfloat16) if t.is_floating_point() else t)(torch.as_tensor(a)))
    def _A_setup():
        B, S, D, H = 1, 2048, 512, 32
        Dh = D // H
        return {
            "x": _w((lambda *s: torch.randn(*s, dtype=torch.bfloat16) * 0.05)(B, S, D)),
            "wq": _w((lambda *s: torch.randn(*s, dtype=torch.bfloat16) * 0.05)(D, H * Dh)),
            "wk": _w((lambda *s: torch.randn(*s, dtype=torch.bfloat16) * 0.05)(D, Dh)),
            "wv": _w((lambda *s: torch.randn(*s, dtype=torch.bfloat16) * 0.05)(D, Dh)),
            "wo": _w((lambda *s: torch.randn(*s, dtype=torch.bfloat16) * 0.05)(H * Dh, D)),
        }
    _A_in = _A_setup()

    def _B_setup():
        B, S, D, F = 1, 2048, 512, 1792
        return {
            "x": _w((lambda *s: torch.randn(*s, dtype=torch.bfloat16) * 0.05)(B, S, D)),
            "W_gate": _w((lambda *s: torch.randn(*s, dtype=torch.bfloat16) * 0.4)(D, F) * (2.0 / D) ** 0.5),
            "W_up": _w((lambda *s: torch.randn(*s, dtype=torch.bfloat16) * 0.4)(D, F) * (2.0 / D) ** 0.5),
            "W_down": _w((lambda *s: torch.randn(*s, dtype=torch.bfloat16) * 0.4)(F, D) * (1.0 / F) ** 0.5),
        }
    _B_in = _B_setup()

    return {
        "A_x": _A_in["x"],
        "A_wq": _A_in["wq"],
        "A_wk": _A_in["wk"],
        "A_wv": _A_in["wv"],
        "A_wo": _A_in["wo"],
        "B_x": _B_in["x"],
        "B_W_gate": _B_in["W_gate"],
        "B_W_up": _B_in["W_up"],
        "B_W_down": _B_in["W_down"],
    }

def reference(A_x, A_wq, A_wk, A_wv, A_wo, B_x, B_W_gate, B_W_up, B_W_down):
    def _A_ref(x, wq, wk, wv, wo):
        B, S, D = x.shape
        H = 32
        Dh = D // H
        q = (lambda x, *d: x.permute(*d))((x @ wq).reshape(B, S, H, Dh), 0, 2, 1, 3)
        k = x @ wk
        v = x @ wv
        scores = torch.einsum("bhqd,bkd->bhqk", q, k) * (Dh ** -0.5)
        p = _softmax(scores, axis=-1)
        o = torch.einsum("bhqk,bkd->bhqd", p, v)
        return (lambda x, *d: x.permute(*d))(o, 0, 2, 1, 3).reshape(B, S, H * Dh) @ wo

    def _B_ref(x, W_gate, W_up, W_down):
        gate = torch.einsum("bsd,df->bsf", x, W_gate)
        up = torch.einsum("bsd,df->bsf", x, W_up)
        gate = gate * (lambda x, dt: x.to(dt))(gate > 0, gate.dtype)
        h = gate * up
        return torch.einsum("bsf,fd->bsd", h, W_down)

    x = A_x
    xn = _layernorm(x, axis=-1)
    return x + _A_ref(xn, A_wq, A_wk, A_wv, A_wo) + _B_ref(xn, B_W_gate, B_W_up, B_W_down)
```

```cpp
#include <hip/hip_runtime.h>
#include <hip/hip_bf16.h>
typedef unsigned short u16;
constexpr int S = 2048, D = 512, H = 32, DH = 16, F = 1792;
__device__ __forceinline__ float b2f(u16 h) { return __uint_as_float((unsigned)h << 16); }
__device__ __forceinline__ u16 f2b(float f) { __hip_bfloat16 b = __float2bfloat16(f); return *reinterpret_cast<u16*>(&b); }

__global__ void k_ln(const u16* x, u16* xn) {
  int row = blockIdx.x, t = threadIdx.x;
  float v[8], s = 0;
  for (int i = 0; i < 8; ++i) { v[i] = b2f(x[row * D + t * 8 + i]); s += v[i]; }
  for (int o = 32; o; o >>= 1) s += __shfl_xor(s, o);
  float m = s / D, q = 0;
  for (int i = 0; i < 8; ++i) { float d = v[i] - m; q += d * d; }
  for (int o = 32; o; o >>= 1) q += __shfl_xor(q, o);
  float r = rsqrtf(q / D + 1e-5f);
  for (int i = 0; i < 8; ++i) xn[row * D + t * 8 + i] = f2b((v[i] - m) * r);
}
__global__ void k_mm(const u16* A, int lda, const u16* B, int ldb, float* C, int ldc, int K, int N) {
  int n = blockIdx.x * 16 + (threadIdx.x & 15), m = blockIdx.y * 16 + (threadIdx.x >> 4);
  float s = 0;
  for (int k = 0; k < K; ++k) s += b2f(A[m * lda + k]) * b2f(B[k * ldb + n]);
  C[m * ldc + n] = s;
}
__global__ void k_glu(const float* g, const float* u, u16* h, int n) {
  int i = blockIdx.x * 256 + threadIdx.x; if (i < n) h[i] = f2b(fmaxf(g[i], 0.f) * u[i]);
}
__global__ void k_attn(const float* q, const float* k, const float* v, u16* o) {
  int idx = blockIdx.x * 256 + threadIdx.x; int row = idx / H, h = idx % H;
  float qq[DH], acc[DH]; for (int d = 0; d < DH; ++d) { qq[d] = q[row * D + h * DH + d] * 0.25f; acc[d] = 0; }
  float m = -INFINITY, l = 0;
  for (int j = 0; j < S; ++j) {
    float s = 0; for (int d = 0; d < DH; ++d) s += qq[d] * k[j * DH + d];
    float mn = fmaxf(m, s), a = __expf(m - mn), p = __expf(s - mn); l = l * a + p;
    for (int d = 0; d < DH; ++d) acc[d] = acc[d] * a + p * v[j * DH + d];
    m = mn;
  }
  for (int d = 0; d < DH; ++d) o[row * D + h * DH + d] = f2b(acc[d] / l);
}
__global__ void k_fin(const u16* x, const float* a, const float* b, u16* out) {
  int i = blockIdx.x * 256 + threadIdx.x; out[i] = f2b(b2f(x[i]) + a[i] + b[i]);
}
extern "C" void kernel_launch(void* const* d_in, const int* in_sizes, int n_in, void* d_out, int out_size, void* d_ws, size_t ws_size, hipStream_t stream) {
  const u16 *x = (const u16*)d_in[0], *wq = (const u16*)d_in[1], *wk = (const u16*)d_in[2], *wv = (const u16*)d_in[3], *wo = (const u16*)d_in[4];
  const u16 *wg = (const u16*)d_in[6], *wu = (const u16*)d_in[7], *wd = (const u16*)d_in[8];
  char* w = (char*)d_ws;
  u16* xn = (u16*)w; w += S * D * 2;
  float* q = (float*)w; w += S * D * 4; float* k = (float*)w; w += S * DH * 4; float* v = (float*)w; w += S * DH * 4;
  float* g = (float*)w; w += S * F * 4; float* u = (float*)w; w += S * F * 4; u16* h = (u16*)w; w += S * F * 2;
  u16* o = (u16*)w; w += S * D * 2; float* a = (float*)w; w += S * D * 4; float* b = (float*)w; w += S * D * 4;
  k_ln<<<S, 64, 0, stream>>>(x, xn);
  k_mm<<<dim3(D / 16, S / 16), 256, 0, stream>>>(xn, D, wq, D, q, D, D, D);
  k_mm<<<dim3(1, S / 16), 256, 0, stream>>>(xn, D, wk, DH, k, DH, D, DH);
  k_mm<<<dim3(1, S / 16), 256, 0, stream>>>(xn, D, wv, DH, v, DH, D, DH);
  k_mm<<<dim3(F / 16, S / 16), 256, 0, stream>>>(xn, D, wg, F, g, F, D, F);
  k_mm<<<dim3(F / 16, S / 16), 256, 0, stream>>>(xn, D, wu, F, u, F, D, F);
  k_glu<<<S * F / 256, 256, 0, stream>>>(g, u, h, S * F);
  k_attn<<<S * H / 256, 256, 0, stream>>>(q, k, v, o);
  k_mm<<<dim3(D / 16, S / 16), 256, 0, stream>>>(o, D, wo, D, a, D, D, D);
  k_mm<<<dim3(D / 16, S / 16), 256, 0, stream>>>(h, F, wd, D, b, D, F, D);
  k_fin<<<S * D / 256, 256, 0, stream>>>(x, a, b, (u16*)d_out);
}
```

```cpp
#include <hip/hip_runtime.h>
#include <hip/hip_bf16.h>

__global__ void __launch_bounds__(512, 2) fused_block_kernel(const void* x, const void* wq, const void* wk, const void* wv, const void* wo,
                                                              const void* wg, const void* wu, const void* wd, void* out, void* ws) {
  __shared__ __attribute__((aligned(1024))) char lds[163840];
  unsigned tid = threadIdx.x;
  unsigned ldsb = (unsigned)(uintptr_t)(__attribute__((address_space(3))) char*)lds;
  asm volatile(
    ".Ltop:\n"
    "s_load_dwordx16 s[4:19], s[0:1], 0x0\n"
    "s_load_dwordx4 s[20:23], s[0:1], 0x40\n"
    "v_and_b32 v1, 63, v0\n"
    "v_lshrrev_b32 v2, 6, v0\n"
    "s_mov_b32 s24, s2\n"
    "v_readfirstlane_b32 s58, v2\n"
    "s_and_b32 s59, s24, 7\n"
    "s_lshr_b32 s60, s24, 3\n"
    "s_lshr_b32 s53, s59, 1\n"
    "s_lshl_b32 s53, s53, 2\n"
    "s_and_b32 s61, s60, 3\n"
    "s_add_u32 s53, s53, s61\n"
    "s_and_b32 s54, s59, 1\n"
    "s_lshl_b32 s54, s54, 3\n"
    "s_lshr_b32 s61, s60, 2\n"
    "s_add_u32 s54, s54, s61\n"
    "s_add_u32 s55, s53, s54\n"
    "s_mul_i32 s55, s55, 4\n"
    "s_and_b32 s55, s55, 7\n"
    "s_and_b32 s52, s58, 3\n"
    "s_and_b32 s59, s54, 7\n"
    "s_cmp_eq_u32 s59, 7\n"
    "s_cselect_b32 s65, 1, 0\n"
    "v_lshlrev_b32 v10, 4, v1\n"
    "s_cmp_lt_u32 s58, 4\n"
    "s_cbranch_scc1 .LA\n"
    "v_lshrrev_b32 v12, 3, v1\n"
    "v_and_b32 v16, 7, v1\n"
    "v_lshrrev_b32 v17, 4, v1\n"
    "v_xor_b32 v16, v16, v17\n"
    "v_lshlrev_b32 v16, 4, v16\n"
    "v_lshl_add_u32 v48, v12, 10, v16\n"
    "v_xor_b32 v49, 64, v48\n"
    "v_add_u32 v49, 0x2000, v49\n"
    "v_add_u32 v50, 0x4000, v48\n"
    "v_add_u32 v51, 0x4000, v49\n"
    "v_lshrrev_b32 v12, 2, v1\n"
    "v_and_b32 v13, 3, v1\n"
    "v_lshlrev_b32 v13, 4, v13\n"
    "v_lshl_add_u32 v54, v12, 10, v13\n"
    "s_lshl_b32 s91, s52, 10\n"
    "s_cmp_lg_u32 s65, 0\n"
    "s_cbranch_scc1 .LG_free\n"
    "s_lshl_b32 s93, s53, 17\n"
    "s_lshl_b32 s60, s52, 15\n"
    "s_add_u32 s93, s93, s60\n"
    "s_lshl_b32 s94, s52, 12\n"
    "s_lshl_b32 s90, s52, 14\n"
    "s_lshl_b32 s59, s54, 6\n"
    "s_add_u32 s90, s90, s59\n"
    "s_lshr_b32 s86, s52, 1\n"
    "s_mul_i32 s86, s86, 6144\n"
    "s_and_b32 s59, s52, 1\n"
    "s_lshl_b32 s59, s59, 10\n"
    "s_add_u32 s86, s86, s59\n"
    "s_waitcnt lgkmcnt(0)\n"
    "s_mov_b32 s36, s6\n"
    "s_and_b32 s37, s7, 0xffff\n"
    "s_mov_b32 s38, 524288\n"
    "s_mov_b32 s39, 0x20000\n"
    "s_mov_b32 s32, s4\n"
    "s_and_b32 s33, s5, 0xffff\n"
    "s_mov_b32 s34, 2097152\n"
    "s_mov_b32 s35, 0x20000\n"
    "s_add_u32 s62, s55, 0\n"
    "s_and_b32 s62, s62, 7\n"
    "s_add_u32 m0, s91, 131072\n"
    "s_lshl_b32 s63, s62, 16\n"
    "s_add_u32 s63, s63, s90\n"
    "buffer_load_dwordx4 v54, s[36:39], s63 offen lds\n"
    "s_add_u32 m0, s94, 0\n"
    "s_lshl_b32 s62, s62, 7\n"
    "s_add_u32 s62, s62, s93\n"
    "buffer_load_dwordx4 v48, s[32:35], s62 offen lds\n"
    "s_add_u32 m0, m0, 0x400\n"
    "s_nop 0\n"
    "buffer_load_dwordx4 v49, s[32:35], s62 offen lds\n"
    "s_add_u32 m0, m0, 0x400\n"
    "s_nop 0\n"
    "buffer_load_dwordx4 v50, s[32:35], s62 offen lds\n"
    "s_add_u32 m0, m0, 0x400\n"
    "s_nop 0\n"
    "buffer_load_dwordx4 v51, s[32:35], s62 offen lds\n"
    "s_add_u32 s62, s55, 1\n"
    "s_and_b32 s62, s62, 7\n"
    "s_add_u32 m0, s91, 137216\n"
    "s_lshl_b32 s63, s62, 16\n"
    "s_add_u32 s63, s63, s90\n"
    "buffer_load_dwordx4 v54, s[36:39], s63 offen lds\n"
    "s_add_u32 m0, s94, 16384\n"
    "s_lshl_b32 s62, s62, 7\n"
    "s_add_u32 s62, s62, s93\n"
    "buffer_load_dwordx4 v48, s[32:35], s62 offen lds\n"
    "s_add_u32 m0, m0, 0x400\n"
    "s_nop 0\n"
    "buffer_load_dwordx4 v49, s[32:35], s62 offen lds\n"
    "s_add_u32 m0, m0, 0x400\n"
    "s_nop 0\n"
    "buffer_load_dwordx4 v50, s[32:35], s62 offen lds\n"
    "s_add_u32 m0, m0, 0x400\n"
    "s_nop 0\n"
    "buffer_load_dwordx4 v51, s[32:35], s62 offen lds\n"
    "s_add_u32 s62, s55, 2\n"
    "s_and_b32 s62, s62, 7\n"
    "s_add_u32 m0, s91, 143360\n"
    "s_lshl_b32 s63, s62, 16\n"
    "s_add_u32 s63, s63, s90\n"
    "buffer_load_dwordx4 v54, s[36:39], s63 offen lds\n"
    "s_add_u32 m0, s94, 32768\n"
    "s_lshl_b32 s62, s62, 7\n"
    "s_add_u32 s62, s62, s93\n"
    "v_and_b32 v6, 31, v1\n"
    "v_lshrrev_b32 v7, 5, v1\n"
    "v_bfe_u32 v8, v1, 2, 2\n"
    "v_and_b32 v9, 3, v1\n"
    "v_lshlrev_b32 v9, 3, v9\n"
    "v_bfe_u32 v11, v1, 4, 1\n"
    "v_lshl_add_u32 v9, v11, 5, v9\n"
    "v_bfe_u32 v12, v6, 1, 3\n"
    "v_xor_b32 v12, v12, v7\n"
    "v_xor_b32 v13, 0, v12\n"
    "v_lshlrev_b32 v13, 4, v13\n"
    "v_lshl_add_u32 v112, v6, 7, v13\n"
    "v_xor_b32 v13, 2, v12\n"
    "v_lshlrev_b32 v13, 4, v13\n"
    "v_lshl_add_u32 v113, v6, 7, v13\n"
    "v_xor_b32 v13, 4, v12\n"
    "v_lshlrev_b32 v13, 4, v13\n"
    "v_lshl_add_u32 v114, v6, 7, v13\n"
    "v_xor_b32 v13, 6, v12\n"
    "v_lshlrev_b32 v13, 4, v13\n"
    "v_lshl_add_u32 v115, v6, 7, v13\n"
    "v_mov_b32 v15, 0x3f803f80\n"
    "v_lshrrev_b32 v12, 2, v1\n"
    "v_and_b32 v13, 3, v1\n"
    "v_lshlrev_b32 v13, 4, v13\n"
    "v_mul_u32_u24 v12, 3584, v12\n"
    "v_add_u32 v56, v12, v13\n"
    "v_lshl_add_u32 v12, v7, 3, v8\n"
    "v_lshl_add_u32 v53, v12, 6, v9\n"
    "v_lshlrev_b32 v14, 3, v1\n"
    "v_mov_b32 v33, 0\n"
    "v_mov_b32 v34, 0\n"
    "v_mov_b32 v35, 0\n"
    "v_mov_b32 v37, 0\n"
    "v_mov_b32 v38, 0\n"
    "v_mov_b32 v39, 0\n"
    "v_mov_b32 v41, 0\n"
    "v_mov_b32 v42, 0\n"
    "v_mov_b32 v43, 0\n"
    "v_mov_b32 v45, 0\n"
    "v_mov_b32 v46, 0\n"
    "v_mov_b32 v47, 0\n"
    "v_mov_b32 v62, 0\n"
    "v_mov_b32 v63, 0\n"
    "v_mov_b32 v118, 0\n"
    "v_mov_b32 v119, 0\n"
    "s_mov_b32 s28, s22\n"
    "s_and_b32 s29, s23, 0xffff\n"
    "s_mov_b32 s30, 10485760\n"
    "s_mov_b32 s31, 0x20000\n"
    "buffer_load_dwordx2 v[4:5], off, s[28:31], 0 sc1\n"
    "s_mov_b32 s40, s14\n"
    "s_and_b32 s41, s15, 0xffff\n"
    "s_mov_b32 s42, 1835008\n"
    "s_mov_b32 s43, 0x20000\n"
    "s_sub_u32 s16, s16, 0x400\n"
    "s_subb_u32 s17, s17, 0\n"
    "s_mov_b32 s44, s16\n"
    "s_and_b32 s45, s17, 0xffff\n"
    "s_mov_b32 s46, 1836032\n"
    "s_mov_b32 s47, 0x20000\n"
    "s_lshr_b32 s59, s54, 3\n"
    "s_sub_u32 s59, s54, s59\n"
    "s_lshl_b32 s59, s59, 2\n"
    "s_add_u32 s88, s59, s52\n"
    "s_lshl_b32 s59, s88, 6\n"
    "v_add_u32 v56, s59, v56\n"
    "v_add_u32 v57, 57344, v56\n"
    "v_add_u32 v58, 114688, v56\n"
    "v_add_u32 v59, 172032, v56\n"
    "s_mul_i32 s87, s52, 6144\n"
    "s_add_u32 s87, s87, 131072\n"
    "v_add_u32 v53, s87, v53\n"
    "s_waitcnt vmcnt(10)\n"
    "buffer_load_dwordx4 v48, s[32:35], s62 offen lds\n"
    "s_add_u32 m0, m0, 0x400\n"
    "s_nop 0\n"
    "s_waitcnt vmcnt(10)\n"
    "buffer_load_dwordx4 v49, s[32:35], s62 offen lds\n"
    "s_add_u32 m0, m0, 0x400\n"
    "s_nop 0\n"
    "s_waitcnt vmcnt(10)\n"
    "buffer_load_dwordx4 v50, s[32:35], s62 offen lds\n"
    "s_add_u32 m0, m0, 0x400\n"
    "s_nop 0\n"
    "s_waitcnt vmcnt(10)\n"
    "buffer_load_dwordx4 v51, s[32:35], s62 offen lds\n"
    "s_barrier\n"
    "s_add_u32 s62, s55, 3\n"
    "s_and_b32 s62, s62, 7\n"
    "s_add_u32 m0, s91, 149504\n"
    "s_lshl_b32 s63, s62, 16\n"
    "s_add_u32 s63, s63, s90\n"
    "s_waitcnt vmcnt(10)\n"
    "buffer_load_dwordx4 v54, s[36:39], s63 offen lds\n"
    "s_add_u32 m0, s94, 49152\n"
    "s_lshl_b32 s62, s62, 7\n"
    "s_add_u32 s62, s62, s93\n"
    "s_waitcnt vmcnt(10)\n"
    "buffer_load_dwordx4 v48, s[32:35], s62 offen lds\n"
    "s_add_u32 m0, m0, 0x400\n"
    "s_nop 0\n"
    "s_waitcnt vmcnt(10)\n"
    "buffer_load_dwordx4 v49, s[32:35], s62 offen lds\n"
    "s_add_u32 m0, m0, 0x400\n"
    "s_nop 0\n"
    "s_waitcnt vmcnt(10)\n"
    "buffer_load_dwordx4 v50, s[32:35], s62 offen lds\n"
    "s_add_u32 m0, m0, 0x400\n"
    "s_nop 0\n"
    "s_waitcnt vmcnt(10)\n"
    "buffer_load_dwordx4 v51, s[32:35], s62 offen lds\n"
    "s_barrier\n"
    "s_add_u32 s62, s55, 4\n"
    "s_and_b32 s62, s62, 7\n"
    "s_add_u32 m0, s86, 135168\n"
    "s_lshl_b32 s63, s62, 16\n"
    "s_add_u32 s63, s63, s90\n"
    "s_waitcnt vmcnt(10)\n"
    "buffer_load_dwordx4 v54, s[36:39], s63 offen lds\n"
    "s_add_u32 m0, s94, 65536\n"
    "s_lshl_b32 s62, s62, 7\n"
    "s_add_u32 s62, s62, s93\n"
    "s_waitcnt vmcnt(10)\n"
    "buffer_load_dwordx4 v48, s[32:35], s62 offen lds\n"
    "s_add_u32 m0, m0, 0x400\n"
    "s_nop 0\n"
    "s_waitcnt vmcnt(10)\n"
    "buffer_load_dwordx4 v49, s[32:35], s62 offen lds\n"
    "s_add_u32 m0, m0, 0x400\n"
    "s_nop 0\n"
    "s_waitcnt vmcnt(10)\n"
    "buffer_load_dwordx4 v50, s[32:35], s62 offen lds\n"
    "s_add_u32 m0, m0, 0x400\n"
    "s_nop 0\n"
    "s_waitcnt vmcnt(10)\n"
    "buffer_load_dwordx4 v51, s[32:35], s62 offen lds\n"
    "s_add_u32 s62, s55, 5\n"
    "s_and_b32 s62, s62, 7\n"
    "s_add_u32 m0, s86, 147456\n"
    "s_lshl_b32 s63, s62, 16\n"
    "s_add_u32 s63, s63, s90\n"
    "s_waitcnt vmcnt(10)\n"
    "buffer_load_dwordx4 v54, s[36:39], s63 offen lds\n"
    "s_barrier\n"
    "s_add_u32 m0, s94, 81920\n"
    "s_lshl_b32 s62, s62, 7\n"
    "s_add_u32 s62, s62, s93\n"
    "s_waitcnt vmcnt(10)\n"
    "buffer_load_dwordx4 v48, s[32:35], s62 offen lds\n"
    "s_add_u32 m0, m0, 0x400\n"
    "s_nop 0\n"
    "s_waitcnt vmcnt(10)\n"
    "buffer_load_dwordx4 v49, s[32:35], s62 offen lds\n"
    "s_add_u32 m0, m0, 0x400\n"
    "s_nop 0\n"
    "s_waitcnt vmcnt(10)\n"
    "buffer_load_dwordx4 v50, s[32:35], s62 offen lds\n"
    "s_add_u32 m0, m0, 0x400\n"
    "s_nop 0\n"
    "s_waitcnt vmcnt(10)\n"
    "buffer_load_dwordx4 v51, s[32:35], s62 offen lds\n"
    "s_add_u32 s62, s55, 6\n"
    "s_and_b32 s62, s62, 7\n"
    "s_add_u32 m0, s91, 155648\n"
    "s_lshl_b32 s63, s62, 16\n"
    "s_add_u32 s63, s63, s90\n"
    "s_waitcnt vmcnt(10)\n"
    "buffer_load_dwordx4 v54, s[36:39], s63 offen lds\n"
    "s_barrier\n"
    "s_add_u32 m0, s94, 98304\n"
    "s_lshl_b32 s62, s62, 7\n"
    "s_add_u32 s62, s62, s93\n"
    "s_waitcnt vmcnt(10)\n"
    "buffer_load_dwordx4 v48, s[32:35], s62 offen lds\n"
    "s_add_u32 m0, m0, 0x400\n"
    "s_nop 0\n"
    "s_waitcnt vmcnt(10)\n"
    "buffer_load_dwordx4 v49, s[32:35], s62 offen lds\n"
    "s_add_u32 m0, m0, 0x400\n"
    "s_nop 0\n"
    "s_waitcnt vmcnt(10)\n"
    "buffer_load_dwordx4 v50, s[32:35], s62 offen lds\n"
    "s_add_u32 m0, m0, 0x400\n"
    "s_nop 0\n"
    "s_waitcnt vmcnt(10)\n"
    "buffer_load_dwordx4 v51, s[32:35], s62 offen lds\n"
    "s_add_u32 s62, s55, 7\n"
    "s_and_b32 s62, s62, 7\n"
    "s_add_u32 m0, s91, 159744\n"
    "s_lshl_b32 s63, s62, 16\n"
    "s_add_u32 s63, s63, s90\n"
    "s_waitcnt vmcnt(10)\n"
    "buffer_load_dwordx4 v54, s[36:39], s63 offen lds\n"
    "s_barrier\n"
    "s_add_u32 m0, s94, 114688\n"
    "s_lshl_b32 s62, s62, 7\n"
    "s_add_u32 s62, s62, s93\n"
    "s_waitcnt vmcnt(10)\n"
    "buffer_load_dwordx4 v48, s[32:35], s62 offen lds\n"
    "s_add_u32 m0, m0, 0x400\n"
    "s_nop 0\n"
    "s_waitcnt vmcnt(10)\n"
    "buffer_load_dwordx4 v49, s[32:35], s62 offen lds\n"
    "s_add_u32 m0, m0, 0x400\n"
    "s_nop 0\n"
    "s_waitcnt vmcnt(10)\n"
    "buffer_load_dwordx4 v50, s[32:35], s62 offen lds\n"
    "s_add_u32 m0, m0, 0x400\n"
    "s_nop 0\n"
    "s_waitcnt vmcnt(10)\n"
    "buffer_load_dwordx4 v51, s[32:35], s62 offen lds\n"
    "s_waitcnt vmcnt(10)\n"
    "s_barrier\n"
    "s_add_u32 s59, s55, 0\n"
    "s_and_b32 s59, s59, 7\n"
    "s_mul_i32 s86, s59, 229376\n"
    "s_add_u32 m0, s87, 0\n"
    "s_nop 0\n"
    "buffer_load_dwordx4 v56, s[40:43], s86 offen lds\n"
    "buffer_load_dwordx4 v56, s[44:47], s86 offen offset:1024 lds\n"
    "s_add_u32 m0, s87, 2048\n"
    "s_nop 0\n"
    "buffer_load_dwordx4 v57, s[40:43], s86 offen lds\n"
    "buffer_load_dwordx4 v57, s[44:47], s86 offen offset:1024 lds\n"
    "s_waitcnt vmcnt(9)\n"
    "s_barrier\n"
    "s_waitcnt vmcnt(4)\n"
    "s_barrier\n"
    "v_readfirstlane_b32 s56, v4\n"
    "v_readfirstlane_b32 s57, v5\n"
    "s_add_u32 s56, s56, 0x7f4a7c15\n"
    "s_addc_u32 s57, s57, 0x9e3779b9\n"
    "v_mov_b32 v12, 156672\n"
    "v_mov_b32 v22, 0\n"
    "v_mov_b32 v23, 0\n"
    "ds_write_b64 v12, v[22:23]\n"
    "ds_write_b64 v12, v[22:23] offset:8\n"
    "ds_write_b64 v12, v[22:23] offset:16\n"
    "ds_write_b64 v12, v[22:23] offset:32\n"
    "ds_write_b64 v12, v[22:23] offset:40\n"
    "s_mov_b32 s36, s12\n"
    "s_and_b32 s37, s13, 0xffff\n"
    "s_mov_b32 s38, 524288\n"
    "s_mov_b32 s39, 0x20000\n"
    "s_mov_b32 s48, s18\n"
    "s_and_b32 s49, s19, 0xffff\n"
    "s_mov_b32 s50, 1835008\n"
    "s_mov_b32 s51, 0x20000\n"
    "s_lshr_b32 s59, s54, 1\n"
    "s_lshl_b32 s59, s59, 7\n"
    "s_lshr_b32 s60, s52, 1\n"
    "s_lshl_b32 s60, s60, 6\n"
    "s_add_u32 s59, s59, s60\n"
    "v_add_u32 v28, s59, v54\n"
    "v_xor_b32 v29, 64, v28\n"
    "s_lshl_b32 s89, s52, 11\n"
    "v_lshl_add_u32 v12, v7, 2, v8\n"
    "v_lshl_add_u32 v12, v12, 6, v9\n"
    "v_add_u32 v26, s89, v12\n"
    "v_add_u32 v25, 0x10000, v26\n"
    "v_add_u32 v22, 0x20000, v26\n"
    "s_and_b32 s61, s54, 1\n"
    "s_lshl_b32 s61, s61, 1\n"
    "s_and_b32 s60, s52, 1\n"
    "s_add_u32 s61, s61, s60\n"
    "s_lshl_b32 s60, s53, 2\n"
    "s_add_u32 s92, s61, s60\n"
    "s_andn2_b32 s91, s92, 1\n"
    "s_lshl_b32 s91, s91, 1\n"
    "s_add_u32 s91, s91, s52\n"
    "s_lshl_b32 s91, s91, 14\n"
    "s_andn2_b32 s59, s59, 64\n"
    "s_add_u32 s91, s91, s59\n"
    "s_add_u32 s74, s91, 0x2000\n"
    "v_lshrrev_b32 v12, 3, v1\n"
    "v_and_b32 v27, 0x70, v10\n"
    "v_lshl_add_u32 v27, v12, 10, v27\n"
    "v_lshrrev_b32 v11, 2, v1\n"
    "v_min_u32 v11, 13, v11\n"
    "v_lshlrev_b32 v11, 7, v11\n"
    "s_lshr_b32 s59, s54, 1\n"
    "s_sub_u32 s59, s59, s55\n"
    "s_and_b32 s59, s59, 7\n"
    "s_lshl_b32 s59, s59, 14\n"
    "s_lshl_b32 s61, s61, 12\n"
    "s_add_u32 s59, s59, s61\n"
    "s_lshr_b32 s60, s52, 1\n"
    "s_lshl_b32 s60, s60, 2\n"
    "v_bfe_u32 v12, v6, 1, 3\n"
    "v_xor_b32 v12, s60, v12\n"
    "v_lshlrev_b32 v12, 4, v12\n"
    "v_lshl_add_u32 v12, v6, 7, v12\n"
    "v_lshl_add_u32 v12, v7, 3, v12\n"
    "v_add_u32 v12, s59, v12\n"
    "v_xor_b32 v13, 0, v12\n"
    "ds_read_b64 v[120:121], v13\n"
    "v_xor_b32 v13, 16, v12\n"
    "ds_read_b64 v[122:123], v13\n"
    "v_xor_b32 v13, 32, v12\n"
    "ds_read_b64 v[124:125], v13\n"
    "v_xor_b32 v13, 48, v12\n"
    "ds_read_b64 v[126:127], v13\n"
    "s_xor_b32 s60, s92, 1\n"
    "s_mul_i32 s61, s52, 28672\n"
    "s_add_u32 s61, s61, 3145728\n"
    "s_mul_i32 s59, s92, 114688\n"
    "s_add_u32 s59, s59, s61\n"
    "v_add_u32 v24, s59, v10\n"
    "s_mul_i32 s59, s60, 114688\n"
    "s_add_u32 s59, s59, s61\n"
    "v_add_u32 v23, s59, v10\n"
    "s_and_b32 s59, s53, 3\n"
    "s_lshl_b32 s59, s59, 1\n"
    "s_and_b32 s60, s54, 7\n"
    "s_add_u32 s95, s59, s60\n"
    "s_cmp_ge_u32 s95, 7\n"
    "s_cselect_b32 s59, 7, 0\n"
    "s_sub_u32 s95, s95, s59\n"
    "s_mul_i32 s93, s52, 458752\n"
    "v_mov_b32 v20, 156672\n"
    "v_mov_b32 v21, 1\n"
    "s_lshl_b32 s59, s88, 7\n"
    "s_lshl_b32 s61, s53, 13\n"
    "s_add_u32 s59, s59, s61\n"
    "s_add_u32 s59, s59, 524288\n"
    "v_not_b32 v16, s56\n"
    "v_not_b32 v17, s57\n"
    "v_lshlrev_b32 v18, 3, v1\n"
    "s_mov_b64 exec, 0xffff\n"
    "buffer_store_dwordx2 v[16:17], v18, s[28:31], s59 offen sc1\n"
    "s_mov_b64 exec, -1\n"
    "ds_read_b128 v[64:67], v112 offset:0\n"
    "ds_read_b128 v[68:71], v112 offset:4096\n"
    "ds_read_b128 v[72:75], v112 offset:8192\n"
    "ds_read_b128 v[76:79], v112 offset:12288\n"
    "s_waitcnt vmcnt(3)\n"
    "ds_read_b64_tr_b16 v[96:97], v53 offset:0\n"
    "ds_read_b64_tr_b16 v[98:99], v53 offset:256\n"
    "ds_read_b64_tr_b16 v[100:101], v53 offset:1024\n"
    "ds_read_b64_tr_b16 v[102:103], v53 offset:1280\n"
    "s_waitcnt lgkmcnt(0)\n"
    "v_mfma_f32_32x32x16_bf16 v[128:143], v[96:99], v[64:67], 0\n"
    "ds_read_b128 v[80:83], v113 offset:0\n"
    "ds_read_b128 v[84:87], v113 offset:4096\n"
    "ds_read_b128 v[88:91], v113 offset:8192\n"
    "ds_read_b128 v[92:95], v113 offset:12288\n"
    "v_mfma_f32_32x32x16_bf16 v[144:159], v[100:103], v[64:67], 0\n"
    "v_mfma_f32_32x32x16_bf16 v[160:175], v[96:99], v[68:71], 0\n"
    "s_add_u32 m0, s87, 0\n"
    "s_nop 0\n"
    "buffer_load_dwordx4 v58, s[40:43], s86 offen lds\n"
    "buffer_load_dwordx4 v58, s[44:47], s86 offen offset:1024 lds\n"
    "v_mfma_f32_32x32x16_bf16 v[176:191], v[100:103], v[68:71], 0\n"
    "v_mfma_f32_32x32x16_bf16 v[192:207], v[96:99], v[72:75], 0\n"
    "s_waitcnt vmcnt(3)\n"
    "ds_read_b64_tr_b16 v[104:105], v53 offset:2048\n"
    "ds_read_b64_tr_b16 v[106:107], v53 offset:2304\n"
    "ds_read_b64_tr_b16 v[108:109], v53 offset:3072\n"
    "ds_read_b64_tr_b16 v[110:111], v53 offset:3328\n"
    "v_mfma_f32_32x32x16_bf16 v[208:223], v[100:103], v[72:75], 0\n"
    "v_mfma_f32_32x32x16_bf16 v[224:239], v[96:99], v[76:79], 0\n"
    "v_mfma_f32_32x32x16_bf16 v[240:255], v[100:103], v[76:79], 0\n"
    "s_waitcnt lgkmcnt(0)\n"
    "v_mfma_f32_32x32x16_bf16 v[128:143], v[104:107], v[80:83], v[128:143]\n"
    "ds_read_b128 v[64:67], v114 offset:0\n"
    "ds_read_b128 v[68:71], v114 offset:4096\n"
    "ds_read_b128 v[72:75], v114 offset:8192\n"
    "ds_read_b128 v[76:79], v114 offset:12288\n"
    "v_mfma_f32_32x32x16_bf16 v[144:159], v[108:111], v[80:83], v[144:159]\n"
    "v_mfma_f32_32x32x16_bf16 v[160:175], v[104:107], v[84:87], v[160:175]\n"
    "s_add_u32 m0, s87, 2048\n"
    "s_nop 0\n"
    "buffer_load_dwordx4 v59, s[40:43], s86 offen lds\n"
    "buffer_load_dwordx4 v59, s[44:47], s86 offen offset:1024 lds\n"
    "v_mfma_f32_32x32x16_bf16 v[176:191], v[108:111], v[84:87], v[176:191]\n"
    "s_barrier\n"
    "s_barrier\n"
    "s_add_u32 s59, s55, 1\n"
    "s_and_b32 s59, s59, 7\n"
    "s_mul_i32 s86, s59, 229376\n"
    "s_add_u32 m0, s87, 4096\n"
    "s_nop 0\n"
    "buffer_load_dwordx4 v56, s[40:43], s86 offen lds\n"
    "buffer_load_dwordx4 v56, s[44:47], s86 offen offset:1024 lds\n"
    "s_setprio 3\n"
    "v_lshlrev_b32 v12, 2, v6\n"
    "v_add_u32 v12, 155648, v12\n"
    "ds_read_b32 v32, v12 offset:0\n"
    "ds_read_b32 v36, v12 offset:128\n"
    "ds_read_b32 v40, v12 offset:256\n"
    "ds_read_b32 v44, v12 offset:384\n"
    "s_and_b32 s59, s54, 1\n"
    "s_lshl_b32 s59, s59, 1\n"
    "s_and_b32 s60, s52, 1\n"
    "s_add_u32 s59, s59, s60\n"
    "s_lshl_b32 s59, s59, 7\n"
    "v_add_u32 v12, s59, v12\n"
    "ds_read_b32 v31, v12 offset:512\n"
    "s_cmp_lg_u32 s52, 0\n"
    "s_cbranch_scc1 .Lsf3\n"
    "s_lshl_b32 s59, s53, 4\n"
    "s_add_u32 s59, s59, s54\n"
    "s_lshl_b32 s59, s59, 7\n"
    "s_add_u32 s59, s59, 32768\n"
    "v_mov_b32 v16, s56\n"
    "v_mov_b32 v17, s57\n"
    "v_lshlrev_b32 v18, 3, v1\n"
    "s_mov_b64 exec, 0xffff\n"
    "buffer_store_dwordx2 v[16:17], v18, s[28:31], s59 offen sc1\n"
    "s_mov_b64 exec, -1\n"
    ".Lsf3:\n"
    "s_setprio 0\n"
    "v_mfma_f32_32x32x16_bf16 v[192:207], v[104:107], v[88:91], v[192:207]\n"
    "s_waitcnt vmcnt(4)\n"
    "ds_read_b64_tr_b16 v[96:97], v53 offset:0\n"
    "ds_read_b64_tr_b16 v[98:99], v53 offset:256\n"
    "ds_read_b64_tr_b16 v[100:101], v53 offset:1024\n"
    "ds_read_b64_tr_b16 v[102:103], v53 offset:1280\n"
    "v_mfma_f32_32x32x16_bf16 v[208:223], v[108:111], v[88:91], v[208:223]\n"
    "v_mfma_f32_32x32x16_bf16 v[224:239], v[104:107], v[92:95], v[224:239]\n"
    "v_mfma_f32_32x32x16_bf16 v[240:255], v[108:111], v[92:95], v[240:255]\n"
    "s_waitcnt lgkmcnt(0)\n"
    "v_mfma_f32_32x32x16_bf16 v[128:143], v[96:99], v[64:67], v[128:143]\n"
    "ds_read_b128 v[80:83], v115 offset:0\n"
    "ds_read_b128 v[84:87], v115 offset:4096\n"
    "ds_read_b128 v[88:91], v115 offset:8192\n"
    "ds_read_b128 v[92:95], v115 offset:12288\n"
    "v_mfma_f32_32x32x16_bf16 v[144:159], v[100:103], v[64:67], v[144:159]\n"
    "v_mfma_f32_32x32x16_bf16 v[160:175], v[96:99], v[68:71], v[160:175]\n"
    "s_add_u32 m0, s87, 0\n"
    "s_nop 0\n"
    "buffer_load_dwordx4 v57, s[40:43], s86 offen lds\n"
    "buffer_load_dwordx4 v57, s[44:47], s86 offen offset:1024 lds\n"
    "v_mfma_f32_32x32x16_bf16 v[176:191], v[100:103], v[68:71], v[176:191]\n"
    "v_mfma_f32_32x32x16_bf16 v[192:207], v[96:99], v[72:75], v[192:207]\n"
    "s_waitcnt vmcnt(4)\n"
    "ds_read_b64_tr_b16 v[104:105], v53 offset:2048\n"
    "ds_read_b64_tr_b16 v[106:107], v53 offset:2304\n"
    "ds_read_b64_tr_b16 v[108:109], v53 offset:3072\n"
    "ds_read_b64_tr_b16 v[110:111], v53 offset:3328\n"
    "v_mfma_f32_32x32x16_bf16 v[208:223], v[100:103], v[72:75], v[208:223]\n"
    "v_mfma_f32_32x32x16_bf16 v[224:239], v[96:99], v[76:79], v[224:239]\n"
    "v_mfma_f32_32x32x16_bf16 v[240:255], v[100:103], v[76:79], v[240:255]\n"
    "s_waitcnt lgkmcnt(0)\n"
    "v_mfma_f32_32x32x16_bf16 v[128:143], v[104:107], v[80:83], v[128:143]\n"
    "ds_read_b128 v[64:67], v112 offset:16384\n"
    "ds_read_b128 v[68:71], v112 offset:20480\n"
    "ds_read_b128 v[72:75], v112 offset:24576\n"
    "ds_read_b128 v[76:79], v112 offset:28672\n"
    "v_mfma_f32_32x32x16_bf16 v[144:159], v[108:111], v[80:83], v[144:159]\n"
    "v_mfma_f32_32x32x16_bf16 v[160:175], v[104:107], v[84:87], v[160:175]\n"
    "s_add_u32 m0, s87, 2048\n"
    "s_nop 0\n"
    "buffer_load_dwordx4 v58, s[40:43], s86 offen lds\n"
    "buffer_load_dwordx4 v58, s[44:47], s86 offen offset:1024 lds\n"
    "v_mfma_f32_32x32x16_bf16 v[176:191], v[108:111], v[84:87], v[176:191]\n"
    "v_mfma_f32_32x32x16_bf16 v[192:207], v[104:107], v[88:91], v[192:207]\n"
    "s_waitcnt vmcnt(4)\n"
    "ds_read_b64_tr_b16 v[96:97], v53 offset:4096\n"
    "ds_read_b64_tr_b16 v[98:99], v53 offset:4352\n"
    "ds_read_b64_tr_b16 v[100:101], v53 offset:5120\n"
    "ds_read_b64_tr_b16 v[102:103], v53 offset:5376\n"
    "v_mfma_f32_32x32x16_bf16 v[208:223], v[108:111], v[88:91], v[208:223]\n"
    "v_mfma_f32_32x32x16_bf16 v[224:239], v[104:107], v[92:95], v[224:239]\n"
    "v_mfma_f32_32x32x16_bf16 v[240:255], v[108:111], v[92:95], v[240:255]\n"
    "s_waitcnt lgkmcnt(0)\n"
    "v_mfma_f32_32x32x16_bf16 v[128:143], v[96:99], v[64:67], v[128:143]\n"
    "ds_read_b128 v[80:83], v113 offset:16384\n"
    "ds_read_b128 v[84:87], v113 offset:20480\n"
    "ds_read_b128 v[88:91], v113 offset:24576\n"
    "ds_read_b128 v[92:95], v113 offset:28672\n"
    "v_mfma_f32_32x32x16_bf16 v[144:159], v[100:103], v[64:67], v[144:159]\n"
    "v_mfma_f32_32x32x16_bf16 v[160:175], v[96:99], v[68:71], v[160:175]\n"
    "s_add_u32 m0, s87, 4096\n"
    "s_nop 0\n"
    "buffer_load_dwordx4 v59, s[40:43], s86 offen lds\n"
    "buffer_load_dwordx4 v59, s[44:47], s86 offen offset:1024 lds\n"
    "v_mfma_f32_32x32x16_bf16 v[176:191], v[100:103], v[68:71], v[176:191]\n"
    "v_mfma_f32_32x32x16_bf16 v[192:207], v[96:99], v[72:75], v[192:207]\n"
    "s_waitcnt vmcnt(4)\n"
    "ds_read_b64_tr_b16 v[104:105], v53 offset:0\n"
    "ds_read_b64_tr_b16 v[106:107], v53 offset:256\n"
    "ds_read_b64_tr_b16 v[108:109], v53 offset:1024\n"
    "ds_read_b64_tr_b16 v[110:111], v53 offset:1280\n"
    "v_mfma_f32_32x32x16_bf16 v[208:223], v[100:103], v[72:75], v[208:223]\n"
    "v_mfma_f32_32x32x16_bf16 v[224:239], v[96:99], v[76:79], v[224:239]\n"
    "v_mfma_f32_32x32x16_bf16 v[240:255], v[100:103], v[76:79], v[240:255]\n"
    "s_waitcnt lgkmcnt(0)\n"
    "v_mfma_f32_32x32x16_bf16 v[128:143], v[104:107], v[80:83], v[128:143]\n"
    "ds_read_b128 v[64:67], v114 offset:16384\n"
    "ds_read_b128 v[68:71], v114 offset:20480\n"
    "ds_read_b128 v[72:75], v114 offset:24576\n"
    "ds_read_b128 v[76:79], v114 offset:28672\n"
    "v_mfma_f32_32x32x16_bf16 v[144:159], v[108:111], v[80:83], v[144:159]\n"
    "v_mfma_f32_32x32x16_bf16 v[160:175], v[104:107], v[84:87], v[160:175]\n"
    "s_add_u32 s59, s55, 2\n"
    "s_and_b32 s59, s59, 7\n"
    "s_mul_i32 s86, s59, 229376\n"
    "s_add_u32 m0, s87, 0\n"
    "s_nop 0\n"
    "buffer_load_dwordx4 v56, s[40:43], s86 offen lds\n"
    "buffer_load_dwordx4 v56, s[44:47], s86 offen offset:1024 lds\n"
    "v_mfma_f32_32x32x16_bf16 v[176:191], v[108:111], v[84:87], v[176:191]\n"
    "v_mfma_f32_32x32x16_bf16 v[192:207], v[104:107], v[88:91], v[192:207]\n"
    "s_waitcnt vmcnt(4)\n"
    "ds_read_b64_tr_b16 v[96:97], v53 offset:2048\n"
    "ds_read_b64_tr_b16 v[98:99], v53 offset:2304\n"
    "ds_read_b64_tr_b16 v[100:101], v53 offset:3072\n"
    "ds_read_b64_tr_b16 v[102:103], v53 offset:3328\n"
    "v_mfma_f32_32x32x16_bf16 v[208:223], v[108:111], v[88:91], v[208:223]\n"
    "v_mfma_f32_32x32x16_bf16 v[224:239], v[104:107], v[92:95], v[224:239]\n"
    "v_mfma_f32_32x32x16_bf16 v[240:255], v[108:111], v[92:95], v[240:255]\n"
    "s_waitcnt lgkmcnt(0)\n"
    "v_mfma_f32_32x32x16_bf16 v[128:143], v[96:99], v[64:67], v[128:143]\n"
    "ds_read_b128 v[80:83], v115 offset:16384\n"
    "ds_read_b128 v[84:87], v115 offset:20480\n"
    "ds_read_b128 v[88:91], v115 offset:24576\n"
    "ds_read_b128 v[92:95], v115 offset:28672\n"
    "v_mfma_f32_32x32x16_bf16 v[144:159], v[100:103], v[64:67], v[144:159]\n"
    "v_mfma_f32_32x32x16_bf16 v[160:175], v[96:99], v[68:71], v[160:175]\n"
    "s_add_u32 m0, s87, 2048\n"
    "s_nop 0\n"
    "buffer_load_dwordx4 v57, s[40:43], s86 offen lds\n"
    "buffer_load_dwordx4 v57, s[44:47], s86 offen offset:1024 lds\n"
    "v_mfma_f32_32x32x16_bf16 v[176:191], v[100:103], v[68:71], v[176:191]\n"
    "v_mfma_f32_32x32x16_bf16 v[192:207], v[96:99], v[72:75], v[192:207]\n"
    "s_waitcnt vmcnt(4)\n"
    "ds_read_b64_tr_b16 v[104:105], v53 offset:4096\n"
    "ds_read_b64_tr_b16 v[106:107], v53 offset:4352\n"
    "ds_read_b64_tr_b16 v[108:109], v53 offset:5120\n"
    "ds_read_b64_tr_b16 v[110:111], v53 offset:5376\n"
    "v_mfma_f32_32x32x16_bf16 v[208:223], v[100:103], v[72:75], v[208:223]\n"
    "v_mfma_f32_32x32x16_bf16 v[224:239], v[96:99], v[76:79], v[224:239]\n"
    "v_mfma_f32_32x32x16_bf16 v[240:255], v[100:103], v[76:79], v[240:255]\n"
    "s_waitcnt lgkmcnt(0)\n"
    "v_mfma_f32_32x32x16_bf16 v[128:143], v[104:107], v[80:83], v[128:143]\n"
    "ds_read_b128 v[64:67], v112 offset:32768\n"
    "ds_read_b128 v[68:71], v112 offset:36864\n"
    "ds_read_b128 v[72:75], v112 offset:40960\n"
    "ds_read_b128 v[76:79], v112 offset:45056\n"
    "v_mfma_f32_32x32x16_bf16 v[144:159], v[108:111], v[80:83], v[144:159]\n"
    "v_mfma_f32_32x32x16_bf16 v[160:175], v[104:107], v[84:87], v[160:175]\n"
    "s_add_u32 m0, s87, 4096\n"
    "s_nop 0\n"
    "buffer_load_dwordx4 v58, s[40:43], s86 offen lds\n"
    "buffer_load_dwordx4 v58, s[44:47], s86 offen offset:1024 lds\n"
    "v_mfma_f32_32x32x16_bf16 v[176:191], v[108:111], v[84:87], v[176:191]\n"
    "v_mfma_f32_32x32x16_bf16 v[192:207], v[104:107], v[88:91], v[192:207]\n"
    "s_waitcnt vmcnt(4)\n"
    "ds_read_b64_tr_b16 v[96:97], v53 offset:0\n"
    "ds_read_b64_tr_b16 v[98:99], v53 offset:256\n"
    "ds_read_b64_tr_b16 v[100:101], v53 offset:1024\n"
    "ds_read_b64_tr_b16 v[102:103], v53 offset:1280\n"
    "v_mfma_f32_32x32x16_bf16 v[208:223], v[108:111], v[88:91], v[208:223]\n"
    "v_mfma_f32_32x32x16_bf16 v[224:239], v[104:107], v[92:95], v[224:239]\n"
    "v_mfma_f32_32x32x16_bf16 v[240:255], v[108:111], v[92:95], v[240:255]\n"
    "s_waitcnt lgkmcnt(0)\n"
    "v_mfma_f32_32x32x16_bf16 v[128:143], v[96:99], v[64:67], v[128:143]\n"
    "ds_read_b128 v[80:83], v113 offset:32768\n"
    "ds_read_b128 v[84:87], v113 offset:36864\n"
    "ds_read_b128 v[88:91], v113 offset:40960\n"
    "ds_read_b128 v[92:95], v113 offset:45056\n"
    "v_mfma_f32_32x32x16_bf16 v[144:159], v[100:103], v[64:67], v[144:159]\n"
    "v_mfma_f32_32x32x16_bf16 v[160:175], v[96:99], v[68:71], v[160:175]\n"
    "s_add_u32 m0, s87, 0\n"
    "s_nop 0\n"
    "buffer_load_dwordx4 v59, s[40:43], s86 offen lds\n"
    "buffer_load_dwordx4 v59, s[44:47], s86 offen offset:1024 lds\n"
    "v_mfma_f32_32x32x16_bf16 v[176:191], v[100:103], v[68:71], v[176:191]\n"
    "v_mfma_f32_32x32x16_bf16 v[192:207], v[96:99], v[72:75], v[192:207]\n"
    "s_waitcnt vmcnt(4)\n"
    "ds_read_b64_tr_b16 v[104:105], v53 offset:2048\n"
    "ds_read_b64_tr_b16 v[106:107], v53 offset:2304\n"
    "ds_read_b64_tr_b16 v[108:109], v53 offset:3072\n"
    "ds_read_b64_tr_b16 v[110:111], v53 offset:3328\n"
    "v_mfma_f32_32x32x16_bf16 v[208:223], v[100:103], v[72:75], v[208:223]\n"
    "v_mfma_f32_32x32x16_bf16 v[224:239], v[96:99], v[76:79], v[224:239]\n"
    "v_mfma_f32_32x32x16_bf16 v[240:255], v[100:103], v[76:79], v[240:255]\n"
    "s_waitcnt lgkmcnt(0)\n"
    "v_mfma_f32_32x32x16_bf16 v[128:143], v[104:107], v[80:83], v[128:143]\n"
    "ds_read_b128 v[64:67], v114 offset:32768\n"
    "ds_read_b128 v[68:71], v114 offset:36864\n"
    "ds_read_b128 v[72:75], v114 offset:40960\n"
    "ds_read_b128 v[76:79], v114 offset:45056\n"
    "v_mfma_f32_32x32x16_bf16 v[144:159], v[108:111], v[80:83], v[144:159]\n"
    "v_mfma_f32_32x32x16_bf16 v[160:175], v[104:107], v[84:87], v[160:175]\n"
    "s_add_u32 s59, s55, 3\n"
    "s_and_b32 s59, s59, 7\n"
    "s_mul_i32 s86, s59, 229376\n"
    "s_add_u32 m0, s87, 2048\n"
    "s_nop 0\n"
    "buffer_load_dwordx4 v56, s[40:43], s86 offen lds\n"
    "buffer_load_dwordx4 v56, s[44:47], s86 offen offset:1024 lds\n"
    "v_mfma_f32_32x32x16_bf16 v[176:191], v[108:111], v[84:87], v[176:191]\n"
    "v_mfma_f32_32x32x16_bf16 v[192:207], v[104:107], v[88:91], v[192:207]\n"
    "s_waitcnt vmcnt(4)\n"
    "ds_read_b64_tr_b16 v[96:97], v53 offset:4096\n"
    "ds_read_b64_tr_b16 v[98:99], v53 offset:4352\n"
    "ds_read_b64_tr_b16 v[100:101], v53 offset:5120\n"
    "ds_read_b64_tr_b16 v[102:103], v53 offset:5376\n"
    "v_mfma_f32_32x32x16_bf16 v[208:223], v[108:111], v[88:91], v[208:223]\n"
    "v_mfma_f32_32x32x16_bf16 v[224:239], v[104:107], v[92:95], v[224:239]\n"
    "v_mfma_f32_32x32x16_bf16 v[240:255], v[108:111], v[92:95], v[240:255]\n"
    "s_waitcnt lgkmcnt(0)\n"
    "v_mfma_f32_32x32x16_bf16 v[128:143], v[96:99], v[64:67], v[128:143]\n"
    "ds_read_b128 v[80:83], v115 offset:32768\n"
    "ds_read_b128 v[84:87], v115 offset:36864\n"
    "ds_read_b128 v[88:91], v115 offset:40960\n"
    "ds_read_b128 v[92:95], v115 offset:45056\n"
    "v_mfma_f32_32x32x16_bf16 v[144:159], v[100:103], v[64:67], v[144:159]\n"
    "v_mfma_f32_32x32x16_bf16 v[160:175], v[96:99], v[68:71], v[160:175]\n"
    "s_add_u32 m0, s87, 4096\n"
    "s_nop 0\n"
    "buffer_load_dwordx4 v57, s[40:43], s86 offen lds\n"
    "buffer_load_dwordx4 v57, s[44:47], s86 offen offset:1024 lds\n"
    "v_mfma_f32_32x32x16_bf16 v[176:191], v[100:103], v[68:71], v[176:191]\n"
    "v_mfma_f32_32x32x16_bf16 v[192:207], v[96:99], v[72:75], v[192:207]\n"
    "s_waitcnt vmcnt(4)\n"
    "ds_read_b64_tr_b16 v[104:105], v53 offset:0\n"
    "ds_read_b64_tr_b16 v[106:107], v53 offset:256\n"
    "ds_read_b64_tr_b16 v[108:109], v53 offset:1024\n"
    "ds_read_b64_tr_b16 v[110:111], v53 offset:1280\n"
    "v_mfma_f32_32x32x16_bf16 v[208:223], v[100:103], v[72:75], v[208:223]\n"
    "v_mfma_f32_32x32x16_bf16 v[224:239], v[96:99], v[76:79], v[224:239]\n"
    "v_mfma_f32_32x32x16_bf16 v[240:255], v[100:103], v[76:79], v[240:255]\n"
    "s_waitcnt lgkmcnt(0)\n"
    "v_mfma_f32_32x32x16_bf16 v[128:143], v[104:107], v[80:83], v[128:143]\n"
    "ds_read_b128 v[64:67], v112 offset:49152\n"
    "ds_read_b128 v[68:71], v112 offset:53248\n"
    "ds_read_b128 v[72:75], v112 offset:57344\n"
    "ds_read_b128 v[76:79], v112 offset:61440\n"
    "v_mfma_f32_32x32x16_bf16 v[144:159], v[108:111], v[80:83], v[144:159]\n"
    "v_mfma_f32_32x32x16_bf16 v[160:175], v[104:107], v[84:87], v[160:175]\n"
    "s_add_u32 m0, s87, 0\n"
    "s_nop 0\n"
    "buffer_load_dwordx4 v58, s[40:43], s86 offen lds\n"
    "buffer_load_dwordx4 v58, s[44:47], s86 offen offset:1024 lds\n"
    "v_mfma_f32_32x32x16_bf16 v[176:191], v[108:111], v[84:87], v[176:191]\n"
    "v_mfma_f32_32x32x16_bf16 v[192:207], v[104:107], v[88:91], v[192:207]\n"
    "s_waitcnt vmcnt(4)\n"
    "ds_read_b64_tr_b16 v[96:97], v53 offset:2048\n"
    "ds_read_b64_tr_b16 v[98:99], v53 offset:2304\n"
    "ds_read_b64_tr_b16 v[100:101], v53 offset:3072\n"
    "ds_read_b64_tr_b16 v[102:103], v53 offset:3328\n"
    "v_mfma_f32_32x32x16_bf16 v[208:223], v[108:111], v[88:91], v[208:223]\n"
    "v_mfma_f32_32x32x16_bf16 v[224:239], v[104:107], v[92:95], v[224:239]\n"
    "v_mfma_f32_32x32x16_bf16 v[240:255], v[108:111], v[92:95], v[240:255]\n"
    "s_waitcnt lgkmcnt(0)\n"
    "v_mfma_f32_32x32x16_bf16 v[128:143], v[96:99], v[64:67], v[128:143]\n"
    "ds_read_b128 v[80:83], v113 offset:49152\n"
    "ds_read_b128 v[84:87], v113 offset:53248\n"
    "ds_read_b128 v[88:91], v113 offset:57344\n"
    "ds_read_b128 v[92:95], v113 offset:61440\n"
    "v_mfma_f32_32x32x16_bf16 v[144:159], v[100:103], v[64:67], v[144:159]\n"
    "v_mfma_f32_32x32x16_bf16 v[160:175], v[96:99], v[68:71], v[160:175]\n"
    "s_add_u32 m0, s87, 2048\n"
    "s_nop 0\n"
    "buffer_load_dwordx4 v59, s[40:43], s86 offen lds\n"
    "buffer_load_dwordx4 v59, s[44:47], s86 offen offset:1024 lds\n"
    "v_mfma_f32_32x32x16_bf16 v[176:191], v[100:103], v[68:71], v[176:191]\n"
    "v_mfma_f32_32x32x16_bf16 v[192:207], v[96:99], v[72:75], v[192:207]\n"
    "s_waitcnt vmcnt(4)\n"
    "ds_read_b64_tr_b16 v[104:105], v53 offset:4096\n"
    "ds_read_b64_tr_b16 v[106:107], v53 offset:4352\n"
    "ds_read_b64_tr_b16 v[108:109], v53 offset:5120\n"
    "ds_read_b64_tr_b16 v[110:111], v53 offset:5376\n"
    "v_mfma_f32_32x32x16_bf16 v[208:223], v[100:103], v[72:75], v[208:223]\n"
    "v_mfma_f32_32x32x16_bf16 v[224:239], v[96:99], v[76:79], v[224:239]\n"
    "v_mfma_f32_32x32x16_bf16 v[240:255], v[100:103], v[76:79], v[240:255]\n"
    "s_waitcnt lgkmcnt(0)\n"
    "v_mfma_f32_32x32x16_bf16 v[128:143], v[104:107], v[80:83], v[128:143]\n"
    "ds_read_b128 v[64:67], v114 offset:49152\n"
    "ds_read_b128 v[68:71], v114 offset:53248\n"
    "ds_read_b128 v[72:75], v114 offset:57344\n"
    "ds_read_b128 v[76:79], v114 offset:61440\n"
    "v_mfma_f32_32x32x16_bf16 v[144:159], v[108:111], v[80:83], v[144:159]\n"
    "v_mfma_f32_32x32x16_bf16 v[160:175], v[104:107], v[84:87], v[160:175]\n"
    "s_add_u32 s59, s55, 4\n"
    "s_and_b32 s59, s59, 7\n"
    "s_mul_i32 s86, s59, 229376\n"
    "s_add_u32 m0, s87, 4096\n"
    "s_nop 0\n"
    "buffer_load_dwordx4 v56, s[40:43], s86 offen lds\n"
    "buffer_load_dwordx4 v56, s[44:47], s86 offen offset:1024 lds\n"
    "v_mfma_f32_32x32x16_bf16 v[176:191], v[108:111], v[84:87], v[176:191]\n"
    "v_mfma_f32_32x32x16_bf16 v[192:207], v[104:107], v[88:91], v[192:207]\n"
    "s_waitcnt vmcnt(4)\n"
    "ds_read_b64_tr_b16 v[96:97], v53 offset:0\n"
    "ds_read_b64_tr_b16 v[98:99], v53 offset:256\n"
    "ds_read_b64_tr_b16 v[100:101], v53 offset:1024\n"
    "ds_read_b64_tr_b16 v[102:103], v53 offset:1280\n"
    "v_mfma_f32_32x32x16_bf16 v[208:223], v[108:111], v[88:91], v[208:223]\n"
    "v_mfma_f32_32x32x16_bf16 v[224:239], v[104:107], v[92:95], v[224:239]\n"
    "v_mfma_f32_32x32x16_bf16 v[240:255], v[108:111], v[92:95], v[240:255]\n"
    "s_waitcnt lgkmcnt(0)\n"
    "v_mfma_f32_32x32x16_bf16 v[128:143], v[96:99], v[64:67], v[128:143]\n"
    "ds_read_b128 v[80:83], v115 offset:49152\n"
    "ds_read_b128 v[84:87], v115 offset:53248\n"
    "ds_read_b128 v[88:91], v115 offset:57344\n"
    "ds_read_b128 v[92:95], v115 offset:61440\n"
    "v_mfma_f32_32x32x16_bf16 v[144:159], v[100:103], v[64:67], v[144:159]\n"
    "v_mfma_f32_32x32x16_bf16 v[160:175], v[96:99], v[68:71], v[160:175]\n"
    "s_add_u32 m0, s87, 0\n"
    "s_nop 0\n"
    "buffer_load_dwordx4 v57, s[40:43], s86 offen lds\n"
    "buffer_load_dwordx4 v57, s[44:47], s86 offen offset:1024 lds\n"
    "v_mfma_f32_32x32x16_bf16 v[176:191], v[100:103], v[68:71], v[176:191]\n"
    "v_mfma_f32_32x32x16_bf16 v[192:207], v[96:99], v[72:75], v[192:207]\n"
    "s_waitcnt vmcnt(4)\n"
    "ds_read_b64_tr_b16 v[104:105], v53 offset:2048\n"
    "ds_read_b64_tr_b16 v[106:107], v53 offset:2304\n"
    "ds_read_b64_tr_b16 v[108:109], v53 offset:3072\n"
    "ds_read_b64_tr_b16 v[110:111], v53 offset:3328\n"
    "v_mfma_f32_32x32x16_bf16 v[208:223], v[100:103], v[72:75], v[208:223]\n"
    "v_mfma_f32_32x32x16_bf16 v[224:239], v[96:99], v[76:79], v[224:239]\n"
    "v_mfma_f32_32x32x16_bf16 v[240:255], v[100:103], v[76:79], v[240:255]\n"
    "s_waitcnt lgkmcnt(0)\n"
    "s_setprio 3\n"
    "v_add_u32 v112, 0x10000, v112\n"
    "v_add_u32 v113, 0x10000, v113\n"
    "v_add_u32 v114, 0x10000, v114\n"
    "v_add_u32 v115, 0x10000, v115\n"
    "s_setprio 0\n"
    "v_mfma_f32_32x32x16_bf16 v[128:143], v[104:107], v[80:83], v[128:143]\n"
    "ds_read_b128 v[64:67], v112 offset:0\n"
    "ds_read_b128 v[68:71], v112 offset:4096\n"
    "ds_read_b128 v[72:75], v112 offset:8192\n"
    "ds_read_b128 v[76:79], v112 offset:12288\n"
    "v_mfma_f32_32x32x16_bf16 v[144:159], v[108:111], v[80:83], v[144:159]\n"
    "v_mfma_f32_32x32x16_bf16 v[160:175], v[104:107], v[84:87], v[160:175]\n"
    "s_add_u32 m0, s87, 2048\n"
    "s_nop 0\n"
    "buffer_load_dwordx4 v58, s[40:43], s86 offen lds\n"
    "buffer_load_dwordx4 v58, s[44:47], s86 offen offset:1024 lds\n"
    "v_mfma_f32_32x32x16_bf16 v[176:191], v[108:111], v[84:87], v[176:191]\n"
    "v_mfma_f32_32x32x16_bf16 v[192:207], v[104:107], v[88:91], v[192:207]\n"
    "s_waitcnt vmcnt(4)\n"
    "ds_read_b64_tr_b16 v[96:97], v53 offset:4096\n"
    "ds_read_b64_tr_b16 v[98:99], v53 offset:4352\n"
    "ds_read_b64_tr_b16 v[100:101], v53 offset:5120\n"
    "ds_read_b64_tr_b16 v[102:103], v53 offset:5376\n"
    "v_mfma_f32_32x32x16_bf16 v[208:223], v[108:111], v[88:91], v[208:223]\n"
    "v_mfma_f32_32x32x16_bf16 v[224:239], v[104:107], v[92:95], v[224:239]\n"
    "s_mov_b64 exec, 1\n"
    "ds_add_u32 v20, v21 offset:16\n"
    "s_mov_b64 exec, -1\n"
    "v_mfma_f32_32x32x16_bf16 v[240:255], v[108:111], v[92:95], v[240:255]\n"
    "s_waitcnt lgkmcnt(0)\n"
    "v_mfma_f32_32x32x16_bf16 v[128:143], v[96:99], v[64:67], v[128:143]\n"
    "ds_read_b128 v[80:83], v113 offset:0\n"
    "ds_read_b128 v[84:87], v113 offset:4096\n"
    "ds_read_b128 v[88:91], v113 offset:8192\n"
    "ds_read_b128 v[92:95], v113 offset:12288\n"
    "v_mfma_f32_32x32x16_bf16 v[144:159], v[100:103], v[64:67], v[144:159]\n"
    "v_mfma_f32_32x32x16_bf16 v[160:175], v[96:99], v[68:71], v[160:175]\n"
    "s_add_u32 m0, s87, 4096\n"
    "s_nop 0\n"
    "buffer_load_dwordx4 v59, s[40:43], s86 offen lds\n"
    "buffer_load_dwordx4 v59, s[44:47], s86 offen offset:1024 lds\n"
    "v_mfma_f32_32x32x16_bf16 v[176:191], v[100:103], v[68:71], v[176:191]\n"
    "v_mfma_f32_32x32x16_bf16 v[192:207], v[96:99], v[72:75], v[192:207]\n"
    "s_waitcnt vmcnt(4)\n"
    "ds_read_b64_tr_b16 v[104:105], v53 offset:0\n"
    "ds_read_b64_tr_b16 v[106:107], v53 offset:256\n"
    "ds_read_b64_tr_b16 v[108:109], v53 offset:1024\n"
    "ds_read_b64_tr_b16 v[110:111], v53 offset:1280\n"
    "v_mfma_f32_32x32x16_bf16 v[208:223], v[100:103], v[72:75], v[208:223]\n"
    "v_mfma_f32_32x32x16_bf16 v[224:239], v[96:99], v[76:79], v[224:239]\n"
    "v_mfma_f32_32x32x16_bf16 v[240:255], v[100:103], v[76:79], v[240:255]\n"
    "s_waitcnt lgkmcnt(0)\n"
    "v_mfma_f32_32x32x16_bf16 v[128:143], v[104:107], v[80:83], v[128:143]\n"
    "ds_read_b128 v[64:67], v114 offset:0\n"
    "ds_read_b128 v[68:71], v114 offset:4096\n"
    "ds_read_b128 v[72:75], v114 offset:8192\n"
    "ds_read_b128 v[76:79], v114 offset:12288\n"
    "v_mfma_f32_32x32x16_bf16 v[144:159], v[108:111], v[80:83], v[144:159]\n"
    "v_mfma_f32_32x32x16_bf16 v[160:175], v[104:107], v[84:87], v[160:175]\n"
    "s_add_u32 s59, s55, 5\n"
    "s_and_b32 s59, s59, 7\n"
    "s_mul_i32 s86, s59, 229376\n"
    "s_add_u32 m0, s87, 0\n"
    "s_nop 0\n"
    "buffer_load_dwordx4 v56, s[40:43], s86 offen lds\n"
    "buffer_load_dwordx4 v56, s[44:47], s86 offen offset:1024 lds\n"
    "v_mfma_f32_32x32x16_bf16 v[176:191], v[108:111], v[84:87], v[176:191]\n"
    "v_mfma_f32_32x32x16_bf16 v[192:207], v[104:107], v[88:91], v[192:207]\n"
    "s_waitcnt vmcnt(4)\n"
    "ds_read_b64_tr_b16 v[96:97], v53 offset:2048\n"
    "ds_read_b64_tr_b16 v[98:99], v53 offset:2304\n"
    "ds_read_b64_tr_b16 v[100:101], v53 offset:3072\n"
    "ds_read_b64_tr_b16 v[102:103], v53 offset:3328\n"
    "v_mfma_f32_32x32x16_bf16 v[208:223], v[108:111], v[88:91], v[208:223]\n"
    "v_mfma_f32_32x32x16_bf16 v[224:239], v[104:107], v[92:95], v[224:239]\n"
    "ds_read_b32 v19, v20 offset:16\n"
    "v_mfma_f32_32x32x16_bf16 v[240:255], v[108:111], v[92:95], v[240:255]\n"
    "s_waitcnt lgkmcnt(0)\n"
    "v_mfma_f32_32x32x16_bf16 v[128:143], v[96:99], v[64:67], v[128:143]\n"
    "ds_read_b128 v[80:83], v115 offset:0\n"
    "ds_read_b128 v[84:87], v115 offset:4096\n"
    "ds_read_b128 v[88:91], v115 offset:8192\n"
    "ds_read_b128 v[92:95], v115 offset:12288\n"
    "v_mfma_f32_32x32x16_bf16 v[144:159], v[100:103], v[64:67], v[144:159]\n"
    "v_mfma_f32_32x32x16_bf16 v[160:175], v[96:99], v[68:71], v[160:175]\n"
    "s_add_u32 m0, s87, 2048\n"
    "s_nop 0\n"
    "buffer_load_dwordx4 v57, s[40:43], s86 offen lds\n"
    "buffer_load_dwordx4 v57, s[44:47], s86 offen offset:1024 lds\n"
    "v_mfma_f32_32x32x16_bf16 v[176:191], v[100:103], v[68:71], v[176:191]\n"
    "v_mfma_f32_32x32x16_bf16 v[192:207], v[96:99], v[72:75], v[192:207]\n"
    "s_waitcnt vmcnt(4)\n"
    "ds_read_b64_tr_b16 v[104:105], v53 offset:4096\n"
    "ds_read_b64_tr_b16 v[106:107], v53 offset:4352\n"
    "ds_read_b64_tr_b16 v[108:109], v53 offset:5120\n"
    "ds_read_b64_tr_b16 v[110:111], v53 offset:5376\n"
    "v_mfma_f32_32x32x16_bf16 v[208:223], v[100:103], v[72:75], v[208:223]\n"
    "v_mfma_f32_32x32x16_bf16 v[224:239], v[96:99], v[76:79], v[224:239]\n"
    "s_setprio 3\n"
    "v_readfirstlane_b32 s59, v19\n"
    "s_cmp_eq_u32 s59, 4\n"
    "s_cbranch_scc1 .LG_mid18\n"
    "v_add_u32 v18, 16, v20\n"
    "s_mov_b32 s67, 400000\n"
    ".Lpoll4:\n"
    "ds_read_b32 v19, v18 offset:0\n"
    "s_waitcnt lgkmcnt(0)\n"
    "v_readfirstlane_b32 s59, v19\n"
    "s_cmp_eq_u32 s59, 4\n"
    "s_cbranch_scc1 .Lpolld4\n"
    "s_sleep 1\n"
    "s_sub_u32 s67, s67, 1\n"
    "s_cbranch_scc0 .Lpoll4\n"
    ".Lpolld4:\n"
    ".LG_mid18:\n"
    "s_setprio 0\n"
    "s_add_u32 s94, s95, 5\n"
    "s_cmp_ge_u32 s94, 7\n"
    "s_cselect_b32 s59, 7, 0\n"
    "s_sub_u32 s94, s94, s59\n"
    "s_lshl_b32 s94, s94, 16\n"
    "s_add_u32 s94, s94, s93\n"
    "s_add_u32 m0, s89, 0\n"
    "s_nop 0\n"
    "buffer_load_dwordx4 v28, s[48:51], s94 offen lds\n"
    "s_add_u32 m0, m0, 0x400\n"
    "s_nop 0\n"
    "buffer_load_dwordx4 v29, s[48:51], s94 offen lds\n"
    "v_mfma_f32_32x32x16_bf16 v[240:255], v[100:103], v[76:79], v[240:255]\n"
    "s_waitcnt lgkmcnt(0)\n"
    "v_mfma_f32_32x32x16_bf16 v[128:143], v[104:107], v[80:83], v[128:143]\n"
    "ds_read_b128 v[64:67], v112 offset:16384\n"
    "ds_read_b128 v[68:71], v112 offset:20480\n"
    "ds_read_b128 v[72:75], v112 offset:24576\n"
    "ds_read_b128 v[76:79], v112 offset:28672\n"
    "v_mfma_f32_32x32x16_bf16 v[144:159], v[108:111], v[80:83], v[144:159]\n"
    "v_mfma_f32_32x32x16_bf16 v[160:175], v[104:107], v[84:87], v[160:175]\n"
    "s_add_u32 m0, s87, 4096\n"
    "s_nop 0\n"
    "buffer_load_dwordx4 v58, s[40:43], s86 offen lds\n"
    "buffer_load_dwordx4 v58, s[44:47], s86 offen offset:1024 lds\n"
    "v_mfma_f32_32x32x16_bf16 v[176:191], v[108:111], v[84:87], v[176:191]\n"
    "v_mfma_f32_32x32x16_bf16 v[192:207], v[104:107], v[88:91], v[192:207]\n"
    "s_waitcnt vmcnt(6)\n"
    "ds_read_b64_tr_b16 v[96:97], v53 offset:0\n"
    "ds_read_b64_tr_b16 v[98:99], v53 offset:256\n"
    "ds_read_b64_tr_b16 v[100:101], v53 offset:1024\n"
    "ds_read_b64_tr_b16 v[102:103], v53 offset:1280\n"
    "v_mfma_f32_32x32x16_bf16 v[208:223], v[108:111], v[88:91], v[208:223]\n"
    "v_mfma_f32_32x32x16_bf16 v[224:239], v[104:107], v[92:95], v[224:239]\n"
    "s_add_u32 s94, s94, 0x4000\n"
    "s_add_u32 m0, s89, 8192\n"
    "s_nop 0\n"
    "buffer_load_dwordx4 v28, s[48:51], s94 offen lds\n"
    "s_add_u32 m0, m0, 0x400\n"
    "s_nop 0\n"
    "buffer_load_dwordx4 v29, s[48:51], s94 offen lds\n"
    "v_mfma_f32_32x32x16_bf16 v[240:255], v[108:111], v[92:95], v[240:255]\n"
    "s_waitcnt lgkmcnt(0)\n"
    "v_mfma_f32_32x32x16_bf16 v[128:143], v[96:99], v[64:67], v[128:143]\n"
    "ds_read_b128 v[80:83], v113 offset:16384\n"
    "ds_read_b128 v[84:87], v113 offset:20480\n"
    "ds_read_b128 v[88:91], v113 offset:24576\n"
    "ds_read_b128 v[92:95], v113 offset:28672\n"
    "v_mfma_f32_32x32x16_bf16 v[144:159], v[100:103], v[64:67], v[144:159]\n"
    "v_mfma_f32_32x32x16_bf16 v[160:175], v[96:99], v[68:71], v[160:175]\n"
    "s_add_u32 m0, s87, 0\n"
    "s_nop 0\n"
    "buffer_load_dwordx4 v59, s[40:43], s86 offen lds\n"
    "buffer_load_dwordx4 v59, s[44:47], s86 offen offset:1024 lds\n"
    "v_mfma_f32_32x32x16_bf16 v[176:191], v[100:103], v[68:71], v[176:191]\n"
    "v_mfma_f32_32x32x16_bf16 v[192:207], v[96:99], v[72:75], v[192:207]\n"
    "s_waitcnt vmcnt(8)\n"
    "ds_read_b64_tr_b16 v[104:105], v53 offset:2048\n"
    "ds_read_b64_tr_b16 v[106:107], v53 offset:2304\n"
    "ds_read_b64_tr_b16 v[108:109], v53 offset:3072\n"
    "ds_read_b64_tr_b16 v[110:111], v53 offset:3328\n"
    "v_mfma_f32_32x32x16_bf16 v[208:223], v[100:103], v[72:75], v[208:223]\n"
    "v_mfma_f32_32x32x16_bf16 v[224:239], v[96:99], v[76:79], v[224:239]\n"
    "s_add_u32 s94, s94, 0x4000\n"
    "s_add_u32 m0, s89, 16384\n"
    "s_waitcnt vmcnt(7)\n"
    "buffer_load_dwordx4 v28, s[48:51], s94 offen lds\n"
    "s_add_u32 m0, m0, 0x400\n"
    "s_waitcnt vmcnt(7)\n"
    "buffer_load_dwordx4 v29, s[48:51], s94 offen lds\n"
    "v_mfma_f32_32x32x16_bf16 v[240:255], v[100:103], v[76:79], v[240:255]\n"
    "s_waitcnt lgkmcnt(0)\n"
    "v_mfma_f32_32x32x16_bf16 v[128:143], v[104:107], v[80:83], v[128:143]\n"
    "ds_read_b128 v[64:67], v114 offset:16384\n"
    "ds_read_b128 v[68:71], v114 offset:20480\n"
    "ds_read_b128 v[72:75], v114 offset:24576\n"
    "ds_read_b128 v[76:79], v114 offset:28672\n"
    "v_mfma_f32_32x32x16_bf16 v[144:159], v[108:111], v[80:83], v[144:159]\n"
    "v_mfma_f32_32x32x16_bf16 v[160:175], v[104:107], v[84:87], v[160:175]\n"
    "s_add_u32 s59, s55, 6\n"
    "s_and_b32 s59, s59, 7\n"
    "s_mul_i32 s86, s59, 229376\n"
    "s_add_u32 m0, s87, 2048\n"
    "s_nop 0\n"
    "buffer_load_dwordx4 v56, s[40:43], s86 offen lds\n"
    "buffer_load_dwordx4 v56, s[44:47], s86 offen offset:1024 lds\n"
    "v_mfma_f32_32x32x16_bf16 v[176:191], v[108:111], v[84:87], v[176:191]\n"
    "v_mfma_f32_32x32x16_bf16 v[192:207], v[104:107], v[88:91], v[192:207]\n"
    "s_waitcnt vmcnt(8)\n"
    "ds_read_b64_tr_b16 v[96:97], v53 offset:4096\n"
    "ds_read_b64_tr_b16 v[98:99], v53 offset:4352\n"
    "ds_read_b64_tr_b16 v[100:101], v53 offset:5120\n"
    "ds_read_b64_tr_b16 v[102:103], v53 offset:5376\n"
    "v_mfma_f32_32x32x16_bf16 v[208:223], v[108:111], v[88:91], v[208:223]\n"
    "v_mfma_f32_32x32x16_bf16 v[224:239], v[104:107], v[92:95], v[224:239]\n"
    "s_add_u32 s94, s94, 0x4000\n"
    "s_add_u32 m0, s89, 24576\n"
    "s_waitcnt vmcnt(7)\n"
    "buffer_load_dwordx4 v28, s[48:51], s94 offen lds\n"
    "s_add_u32 m0, m0, 0x400\n"
    "s_waitcnt vmcnt(7)\n"
    "buffer_load_dwordx4 v29, s[48:51], s94 offen lds\n"
    "v_mfma_f32_32x32x16_bf16 v[240:255], v[108:111], v[92:95], v[240:255]\n"
    "s_waitcnt lgkmcnt(0)\n"
    "v_mfma_f32_32x32x16_bf16 v[128:143], v[96:99], v[64:67], v[128:143]\n"
    "ds_read_b128 v[80:83], v115 offset:16384\n"
    "ds_read_b128 v[84:87], v115 offset:20480\n"
    "ds_read_b128 v[88:91], v115 offset:24576\n"
    "ds_read_b128 v[92:95], v115 offset:28672\n"
    "v_mfma_f32_32x32x16_bf16 v[144:159], v[100:103], v[64:67], v[144:159]\n"
    "v_mfma_f32_32x32x16_bf16 v[160:175], v[96:99], v[68:71], v[160:175]\n"
    "s_add_u32 m0, s87, 4096\n"
    "s_nop 0\n"
    "buffer_load_dwordx4 v57, s[40:43], s86 offen lds\n"
    "buffer_load_dwordx4 v57, s[44:47], s86 offen offset:1024 lds\n"
    "v_mfma_f32_32x32x16_bf16 v[176:191], v[100:103], v[68:71], v[176:191]\n"
    "v_mfma_f32_32x32x16_bf16 v[192:207], v[96:99], v[72:75], v[192:207]\n"
    "s_waitcnt vmcnt(8)\n"
    "ds_read_b64_tr_b16 v[104:105], v53 offset:0\n"
    "ds_read_b64_tr_b16 v[106:107], v53 offset:256\n"
    "ds_read_b64_tr_b16 v[108:109], v53 offset:1024\n"
    "ds_read_b64_tr_b16 v[110:111], v53 offset:1280\n"
    "v_mfma_f32_32x32x16_bf16 v[208:223], v[100:103], v[72:75], v[208:223]\n"
    "v_mfma_f32_32x32x16_bf16 v[224:239], v[96:99], v[76:79], v[224:239]\n"
    "s_add_u32 s94, s95, 6\n"
    "s_cmp_ge_u32 s94, 7\n"
    "s_cselect_b32 s59, 7, 0\n"
    "s_sub_u32 s94, s94, s59\n"
    "s_lshl_b32 s94, s94, 16\n"
    "s_add_u32 s94, s94, s93\n"
    "s_add_u32 m0, s89, 32768\n"
    "s_waitcnt vmcnt(7)\n"
    "buffer_load_dwordx4 v28, s[48:51], s94 offen lds\n"
    "s_add_u32 m0, m0, 0x400\n"
    "s_waitcnt vmcnt(7)\n"
    "buffer_load_dwordx4 v29, s[48:51], s94 offen lds\n"
    "v_mfma_f32_32x32x16_bf16 v[240:255], v[100:103], v[76:79], v[240:255]\n"
    "s_waitcnt lgkmcnt(0)\n"
    "v_mfma_f32_32x32x16_bf16 v[128:143], v[104:107], v[80:83], v[128:143]\n"
    "ds_read_b128 v[64:67], v112 offset:32768\n"
    "ds_read_b128 v[68:71], v112 offset:36864\n"
    "ds_read_b128 v[72:75], v112 offset:40960\n"
    "ds_read_b128 v[76:79], v112 offset:45056\n"
    "v_mfma_f32_32x32x16_bf16 v[144:159], v[108:111], v[80:83], v[144:159]\n"
    "v_mfma_f32_32x32x16_bf16 v[160:175], v[104:107], v[84:87], v[160:175]\n"
    "s_add_u32 m0, s87, 0\n"
    "s_nop 0\n"
    "buffer_load_dwordx4 v58, s[40:43], s86 offen lds\n"
    "buffer_load_dwordx4 v58, s[44:47], s86 offen offset:1024 lds\n"
    "v_mfma_f32_32x32x16_bf16 v[176:191], v[108:111], v[84:87], v[176:191]\n"
    "v_mfma_f32_32x32x16_bf16 v[192:207], v[104:107], v[88:91], v[192:207]\n"
    "s_waitcnt vmcnt(8)\n"
    "ds_read_b64_tr_b16 v[96:97], v53 offset:2048\n"
    "ds_read_b64_tr_b16 v[98:99], v53 offset:2304\n"
    "ds_read_b64_tr_b16 v[100:101], v53 offset:3072\n"
    "ds_read_b64_tr_b16 v[102:103], v53 offset:3328\n"
    "v_mfma_f32_32x32x16_bf16 v[208:223], v[108:111], v[88:91], v[208:223]\n"
    "v_mfma_f32_32x32x16_bf16 v[224:239], v[104:107], v[92:95], v[224:239]\n"
    "s_mov_b64 exec, 1\n"
    "ds_add_u32 v20, v21 offset:20\n"
    "s_mov_b64 exec, -1\n"
    "s_add_u32 s94, s94, 0x4000\n"
    "s_add_u32 m0, s89, 40960\n"
    "s_waitcnt vmcnt(7)\n"
    "buffer_load_dwordx4 v28, s[48:51], s94 offen lds\n"
    "s_add_u32 m0, m0, 0x400\n"
    "s_waitcnt vmcnt(7)\n"
    "buffer_load_dwordx4 v29, s[48:51], s94 offen lds\n"
    "v_mfma_f32_32x32x16_bf16 v[240:255], v[108:111], v[92:95], v[240:255]\n"
    "s_waitcnt lgkmcnt(0)\n"
    "v_mfma_f32_32x32x16_bf16 v[128:143], v[96:99], v[64:67], v[128:143]\n"
    "ds_read_b128 v[80:83], v113 offset:32768\n"
    "ds_read_b128 v[84:87], v113 offset:36864\n"
    "ds_read_b128 v[88:91], v113 offset:40960\n"
    "ds_read_b128 v[92:95], v113 offset:45056\n"
    "v_mfma_f32_32x32x16_bf16 v[144:159], v[100:103], v[64:67], v[144:159]\n"
    "v_mfma_f32_32x32x16_bf16 v[160:175], v[96:99], v[68:71], v[160:175]\n"
    "s_add_u32 m0, s87, 2048\n"
    "s_nop 0\n"
    "buffer_load_dwordx4 v59, s[40:43], s86 offen lds\n"
    "buffer_load_dwordx4 v59, s[44:47], s86 offen offset:1024 lds\n"
    "v_mfma_f32_32x32x16_bf16 v[176:191], v[100:103], v[68:71], v[176:191]\n"
    "v_mfma_f32_32x32x16_bf16 v[192:207], v[96:99], v[72:75], v[192:207]\n"
    "s_waitcnt vmcnt(8)\n"
    "ds_read_b64_tr_b16 v[104:105], v53 offset:4096\n"
    "ds_read_b64_tr_b16 v[106:107], v53 offset:4352\n"
    "ds_read_b64_tr_b16 v[108:109], v53 offset:5120\n"
    "ds_read_b64_tr_b16 v[110:111], v53 offset:5376\n"
    "v_mfma_f32_32x32x16_bf16 v[208:223], v[100:103], v[72:75], v[208:223]\n"
    "v_mfma_f32_32x32x16_bf16 v[224:239], v[96:99], v[76:79], v[224:239]\n"
    "s_add_u32 s94, s94, 0x4000\n"
    "s_add_u32 m0, s89, 49152\n"
    "s_waitcnt vmcnt(7)\n"
    "buffer_load_dwordx4 v28, s[48:51], s94 offen lds\n"
    "s_add_u32 m0, m0, 0x400\n"
    "s_waitcnt vmcnt(7)\n"
    "buffer_load_dwordx4 v29, s[48:51], s94 offen lds\n"
    "v_mfma_f32_32x32x16_bf16 v[240:255], v[100:103], v[76:79], v[240:255]\n"
    "s_waitcnt lgkmcnt(0)\n"
    "v_mfma_f32_32x32x16_bf16 v[128:143], v[104:107], v[80:83], v[128:143]\n"
    "ds_read_b128 v[64:67], v114 offset:32768\n"
    "ds_read_b128 v[68:71], v114 offset:36864\n"
    "ds_read_b128 v[72:75], v114 offset:40960\n"
    "ds_read_b128 v[76:79], v114 offset:45056\n"
    "v_mfma_f32_32x32x16_bf16 v[144:159], v[108:111], v[80:83], v[144:159]\n"
    "v_mfma_f32_32x32x16_bf16 v[160:175], v[104:107], v[84:87], v[160:175]\n"
    "s_add_u32 s59, s55, 7\n"
    "s_and_b32 s59, s59, 7\n"
    "s_mul_i32 s86, s59, 229376\n"
    "s_add_u32 m0, s87, 4096\n"
    "s_nop 0\n"
    "buffer_load_dwordx4 v56, s[40:43], s86 offen lds\n"
    "buffer_load_dwordx4 v56, s[44:47], s86 offen offset:1024 lds\n"
    "v_mfma_f32_32x32x16_bf16 v[176:191], v[108:111], v[84:87], v[176:191]\n"
    "v_mfma_f32_32x32x16_bf16 v[192:207], v[104:107], v[88:91], v[192:207]\n"
    "s_waitcnt vmcnt(8)\n"
    "ds_read_b64_tr_b16 v[96:97], v53 offset:0\n"
    "ds_read_b64_tr_b16 v[98:99], v53 offset:256\n"
    "ds_read_b64_tr_b16 v[100:101], v53 offset:1024\n"
    "ds_read_b64_tr_b16 v[102:103], v53 offset:1280\n"
    "v_mfma_f32_32x32x16_bf16 v[208:223], v[108:111], v[88:91], v[208:223]\n"
    "v_mfma_f32_32x32x16_bf16 v[224:239], v[104:107], v[92:95], v[224:239]\n"
    "ds_read_b32 v19, v20 offset:20\n"
    "s_add_u32 s94, s94, 0x4000\n"
    "s_add_u32 m0, s89, 57344\n"
    "s_waitcnt vmcnt(7)\n"
    "buffer_load_dwordx4 v28, s[48:51], s94 offen lds\n"
    "s_add_u32 m0, m0, 0x400\n"
    "s_waitcnt vmcnt(7)\n"
    "buffer_load_dwordx4 v29, s[48:51], s94 offen lds\n"
    "v_mfma_f32_32x32x16_bf16 v[240:255], v[108:111], v[92:95], v[240:255]\n"
    "s_waitcnt lgkmcnt(0)\n"
    "v_mfma_f32_32x32x16_bf16 v[128:143], v[96:99], v[64:67], v[128:143]\n"
    "ds_read_b128 v[80:83], v115 offset:32768\n"
    "ds_read_b128 v[84:87], v115 offset:36864\n"
    "ds_read_b128 v[88:91], v115 offset:40960\n"
    "ds_read_b128 v[92:95], v115 offset:45056\n"
    "v_mfma_f32_32x32x16_bf16 v[144:159], v[100:103], v[64:67], v[144:159]\n"
    "v_mfma_f32_32x32x16_bf16 v[160:175], v[96:99], v[68:71], v[160:175]\n"
    "s_add_u32 m0, s87, 0\n"
    "s_nop 0\n"
    "buffer_load_dwordx4 v57, s[40:43], s86 offen lds\n"
    "buffer_load_dwordx4 v57, s[44:47], s86 offen offset:1024 lds\n"
    "v_mfma_f32_32x32x16_bf16 v[176:191], v[100:103], v[68:71], v[176:191]\n"
    "v_mfma_f32_32x32x16_bf16 v[192:207], v[96:99], v[72:75], v[192:207]\n"
    "s_waitcnt vmcnt(8)\n"
    "ds_read_b64_tr_b16 v[104:105], v53 offset:2048\n"
    "ds_read_b64_tr_b16 v[106:107], v53 offset:2304\n"
    "ds_read_b64_tr_b16 v[108:109], v53 offset:3072\n"
    "ds_read_b64_tr_b16 v[110:111], v53 offset:3328\n"
    "v_mfma_f32_32x32x16_bf16 v[208:223], v[100:103], v[72:75], v[208:223]\n"
    "v_mfma_f32_32x32x16_bf16 v[224:239], v[96:99], v[76:79], v[224:239]\n"
    "s_setprio 3\n"
    "v_readfirstlane_b32 s59, v19\n"
    "s_cmp_eq_u32 s59, 4\n"
    "s_cbranch_scc1 .LG_mid26\n"
    "v_add_u32 v18, 20, v20\n"
    "s_mov_b32 s67, 400000\n"
    ".Lpoll5:\n"
    "ds_read_b32 v19, v18 offset:0\n"
    "s_waitcnt lgkmcnt(0)\n"
    "v_readfirstlane_b32 s59, v19\n"
    "s_cmp_eq_u32 s59, 4\n"
    "s_cbranch_scc1 .Lpolld5\n"
    "s_sleep 1\n"
    "s_sub_u32 s67, s67, 1\n"
    "s_cbranch_scc0 .Lpoll5\n"
    ".Lpolld5:\n"
    ".LG_mid26:\n"
    "s_setprio 0\n"
    "s_add_u32 s94, s95, 0\n"
    "s_cmp_ge_u32 s94, 7\n"
    "s_cselect_b32 s59, 7, 0\n"
    "s_sub_u32 s94, s94, s59\n"
    "s_lshl_b32 s94, s94, 16\n"
    "s_add_u32 s94, s94, s93\n"
    "s_add_u32 m0, s89, 65536\n"
    "s_waitcnt vmcnt(7)\n"
    "buffer_load_dwordx4 v28, s[48:51], s94 offen lds\n"
    "s_add_u32 m0, m0, 0x400\n"
    "s_waitcnt vmcnt(7)\n"
    "buffer_load_dwordx4 v29, s[48:51], s94 offen lds\n"
    "s_lshl_b32 s90, s88, 10\n"
    "s_add_u32 s90, s90, 786432\n"
    "buffer_load_dwordx2 v[60:61], v14, s[28:31], s90 offen sc1\n"
    "buffer_load_dwordx2 v[116:117], v14, s[28:31], s90 offen offset:512 sc1\n"
    "v_mfma_f32_32x32x16_bf16 v[240:255], v[100:103], v[76:79], v[240:255]\n"
    "s_waitcnt lgkmcnt(0)\n"
    "v_mfma_f32_32x32x16_bf16 v[128:143], v[104:107], v[80:83], v[128:143]\n"
    "ds_read_b128 v[64:67], v112 offset:49152\n"
    "ds_read_b128 v[68:71], v112 offset:53248\n"
    "ds_read_b128 v[72:75], v112 offset:57344\n"
    "ds_read_b128 v[76:79], v112 offset:61440\n"
    "v_mfma_f32_32x32x16_bf16 v[144:159], v[108:111], v[80:83], v[144:159]\n"
    "v_mfma_f32_32x32x16_bf16 v[160:175], v[104:107], v[84:87], v[160:175]\n"
    "s_add_u32 m0, s87, 2048\n"
    "s_nop 0\n"
    "buffer_load_dwordx4 v58, s[40:43], s86 offen lds\n"
    "buffer_load_dwordx4 v58, s[44:47], s86 offen offset:1024 lds\n"
    "v_mfma_f32_32x32x16_bf16 v[176:191], v[108:111], v[84:87], v[176:191]\n"
    "v_mfma_f32_32x32x16_bf16 v[192:207], v[104:107], v[88:91], v[192:207]\n"
    "s_waitcnt vmcnt(10)\n"
    "ds_read_b64_tr_b16 v[96:97], v53 offset:4096\n"
    "ds_read_b64_tr_b16 v[98:99], v53 offset:4352\n"
    "ds_read_b64_tr_b16 v[100:101], v53 offset:5120\n"
    "ds_read_b64_tr_b16 v[102:103], v53 offset:5376\n"
    "v_mfma_f32_32x32x16_bf16 v[208:223], v[108:111], v[88:91], v[208:223]\n"
    "v_mfma_f32_32x32x16_bf16 v[224:239], v[104:107], v[92:95], v[224:239]\n"
    "s_add_u32 s94, s94, 0x4000\n"
    "s_add_u32 m0, s89, 73728\n"
    "s_waitcnt vmcnt(7)\n"
    "buffer_load_dwordx4 v28, s[48:51], s94 offen lds\n"
    "s_add_u32 m0, m0, 0x400\n"
    "s_waitcnt vmcnt(7)\n"
    "buffer_load_dwordx4 v29, s[48:51], s94 offen lds\n"
    "v_mfma_f32_32x32x16_bf16 v[240:255], v[108:111], v[92:95], v[240:255]\n"
    "s_waitcnt lgkmcnt(0)\n"
    "v_mfma_f32_32x32x16_bf16 v[128:143], v[96:99], v[64:67], v[128:143]\n"
    "ds_read_b128 v[80:83], v113 offset:49152\n"
    "ds_read_b128 v[84:87], v113 offset:53248\n"
    "ds_read_b128 v[88:91], v113 offset:57344\n"
    "ds_read_b128 v[92:95], v113 offset:61440\n"
    "v_mfma_f32_32x32x16_bf16 v[144:159], v[100:103], v[64:67], v[144:159]\n"
    "v_mfma_f32_32x32x16_bf16 v[160:175], v[96:99], v[68:71], v[160:175]\n"
    "s_add_u32 m0, s87, 4096\n"
    "s_nop 0\n"
    "buffer_load_dwordx4 v59, s[40:43], s86 offen lds\n"
    "buffer_load_dwordx4 v59, s[44:47], s86 offen offset:1024 lds\n"
    "v_mfma_f32_32x32x16_bf16 v[176:191], v[100:103], v[68:71], v[176:191]\n"
    "v_mfma_f32_32x32x16_bf16 v[192:207], v[96:99], v[72:75], v[192:207]\n"
    "ds_read_b64_tr_b16 v[104:105], v53 offset:0\n"
    "ds_read_b64_tr_b16 v[106:107], v53 offset:256\n"
    "ds_read_b64_tr_b16 v[108:109], v53 offset:1024\n"
    "ds_read_b64_tr_b16 v[110:111], v53 offset:1280\n"
    "v_mfma_f32_32x32x16_bf16 v[208:223], v[100:103], v[72:75], v[208:223]\n"
    "v_mfma_f32_32x32x16_bf16 v[224:239], v[96:99], v[76:79], v[224:239]\n"
    "s_add_u32 s94, s94, 0x4000\n"
    "s_add_u32 m0, s89, 81920\n"
    "s_waitcnt vmcnt(7)\n"
    "buffer_load_dwordx4 v28, s[48:51], s94 offen lds\n"
    "s_add_u32 m0, m0, 0x400\n"
    "s_waitcnt vmcnt(7)\n"
    "buffer_load_dwordx4 v29, s[48:51], s94 offen lds\n"
    "v_mfma_f32_32x32x16_bf16 v[240:255], v[100:103], v[76:79], v[240:255]\n"
    "s_waitcnt lgkmcnt(0)\n"
    "v_mfma_f32_32x32x16_bf16 v[128:143], v[104:107], v[80:83], v[128:143]\n"
    "ds_read_b128 v[64:67], v114 offset:49152\n"
    "ds_read_b128 v[68:71], v114 offset:53248\n"
    "ds_read_b128 v[72:75], v114 offset:57344\n"
    "ds_read_b128 v[76:79], v114 offset:61440\n"
    "v_mfma_f32_32x32x16_bf16 v[144:159], v[108:111], v[80:83], v[144:159]\n"
    "v_mfma_f32_32x32x16_bf16 v[160:175], v[104:107], v[84:87], v[160:175]\n"
    "v_mfma_f32_32x32x16_bf16 v[176:191], v[108:111], v[84:87], v[176:191]\n"
    "v_mfma_f32_32x32x16_bf16 v[192:207], v[104:107], v[88:91], v[192:207]\n"
    "s_waitcnt vmcnt(6)\n"
    "ds_read_b64_tr_b16 v[96:97], v53 offset:2048\n"
    "ds_read_b64_tr_b16 v[98:99], v53 offset:2304\n"
    "ds_read_b64_tr_b16 v[100:101], v53 offset:3072\n"
    "ds_read_b64_tr_b16 v[102:103], v53 offset:3328\n"
    "v_mfma_f32_32x32x16_bf16 v[208:223], v[108:111], v[88:91], v[208:223]\n"
    "v_mfma_f32_32x32x16_bf16 v[224:239], v[104:107], v[92:95], v[224:239]\n"
    "s_add_u32 s94, s94, 0x4000\n"
    "s_add_u32 m0, s89, 90112\n"
    "s_nop 0\n"
    "buffer_load_dwordx4 v28, s[48:51], s94 offen lds\n"
    "s_add_u32 m0, m0, 0x400\n"
    "s_nop 0\n"
    "buffer_load_dwordx4 v29, s[48:51], s94 offen lds\n"
    "v_mfma_f32_32x32x16_bf16 v[240:255], v[108:111], v[92:95], v[240:255]\n"
    "s_waitcnt lgkmcnt(0)\n"
    ".LG_skip1:\n"
    "v_mfma_f32_32x32x16_bf16 v[128:143], v[96:99], v[64:67], v[128:143]\n"
    "ds_read_b128 v[80:83], v115 offset:49152\n"
    "ds_read_b128 v[84:87], v115 offset:53248\n"
    "ds_read_b128 v[88:91], v115 offset:57344\n"
    "ds_read_b128 v[92:95], v115 offset:61440\n"
    "s_waitcnt vmcnt(4)\n"
    "ds_read_b64_tr_b16 v[104:105], v53 offset:4096\n"
    "ds_read_b64_tr_b16 v[106:107], v53 offset:4352\n"
    "ds_read_b64_tr_b16 v[108:109], v53 offset:5120\n"
    "ds_read_b64_tr_b16 v[110:111], v53 offset:5376\n"
    "s_mul_i32 s62, s53, 458752\n"
    "s_lshl_b32 s61, s88, 11\n"
    "s_add_u32 s62, s62, s61\n"
    "s_add_u32 s62, s62, 3145728\n"
    "v_mfma_f32_32x32x16_bf16 v[144:159], v[100:103], v[64:67], v[144:159]\n"
    "s_waitcnt lgkmcnt(0)\n"
    "s_mov_b64 exec, 1\n"
    "ds_add_u32 v20, v21 offset:0\n"
    "s_mov_b64 exec, -1\n"
    "v_mfma_f32_32x32x16_bf16 v[128:143], v[104:107], v[80:83], v[128:143]\n"
    "v_mfma_f32_32x32x16_bf16 v[144:159], v[108:111], v[80:83], v[144:159]\n"
    "s_setprio 3\n"
    "v_cmp_ne_u32 vcc, s56, v61\n"
    "v_cmp_ne_u32 s[60:61], s56, v117\n"
    "s_nop 1\n"
    "s_or_b64 vcc, vcc, s[60:61]\n"
    "s_cbranch_scc1 .LG_cs\n"
    ".LG_csd:\n"
    "v_mov_b32 v61, 0\n"
    "v_mov_b32 v117, 0\n"
    "s_setprio 0\n"
    "s_nop 0\n"
    "v_mfma_f32_32x32x16_bf16 v[128:143], v[60:63], v[32:35], v[128:143]\n"
    "v_mfma_f32_32x32x16_bf16 v[144:159], v[116:119], v[32:35], v[144:159]\n"
    "v_mfma_f32_32x32x16_bf16 v[160:175], v[96:99], v[68:71], v[160:175]\n"
    "v_mfma_f32_32x32x16_bf16 v[176:191], v[100:103], v[68:71], v[176:191]\n"
    "s_nop 8\n"
    "s_setprio 3\n"
    "v_max_f32 v128, 0, v128\n"
    "v_max_f32 v129, 0, v129\n"
    "v_max_f32 v130, 0, v130\n"
    "v_max_f32 v131, 0, v131\n"
    "v_max_f32 v132, 0, v132\n"
    "v_max_f32 v133, 0, v133\n"
    "v_max_f32 v134, 0, v134\n"
    "v_max_f32 v135, 0, v135\n"
    "v_mul_f32 v128, v128, v144\n"
    "s_setprio 0\n"
    "v_mfma_f32_32x32x16_bf16 v[160:175], v[104:107], v[84:87], v[160:175]\n"
    "s_setprio 3\n"
    "v_mul_f32 v129, v129, v145\n"
    "v_mul_f32 v130, v130, v146\n"
    "v_mul_f32 v131, v131, v147\n"
    "v_mul_f32 v132, v132, v148\n"
    "v_mul_f32 v133, v133, v149\n"
    "v_mul_f32 v134, v134, v150\n"
    "v_mul_f32 v135, v135, v151\n"
    "v_cvt_pk_bf16_f32 v144, v128, v129\n"
    "v_cvt_pk_bf16_f32 v145, v130, v131\n"
    "s_setprio 0\n"
    "v_mfma_f32_32x32x16_bf16 v[176:191], v[108:111], v[84:87], v[176:191]\n"
    "ds_read_b32 v19, v20\n"
    "s_setprio 3\n"
    "v_cvt_pk_bf16_f32 v146, v132, v133\n"
    "v_cvt_pk_bf16_f32 v147, v134, v135\n"
    "buffer_store_dwordx4 v[144:147], v10, s[28:31], s62 offen offset:0 sc1\n"
    "v_max_f32 v136, 0, v136\n"
    "v_max_f32 v137, 0, v137\n"
    "v_max_f32 v138, 0, v138\n"
    "v_max_f32 v139, 0, v139\n"
    "v_max_f32 v140, 0, v140\n"
    "v_max_f32 v141, 0, v141\n"
    "s_setprio 0\n"
    "v_mfma_f32_32x32x16_bf16 v[160:175], v[60:63], v[36:39], v[160:175]\n"
    "s_waitcnt lgkmcnt(0)\n"
    "s_setprio 3\n"
    "v_readfirstlane_b32 s59, v19\n"
    "s_cmp_eq_u32 s59, 4\n"
    "s_cbranch_scc1 .LG_fin\n"
    "s_mov_b32 s67, 400000\n"
    ".Lpoll6:\n"
    "ds_read_b32 v19, v20 offset:0\n"
    "s_waitcnt lgkmcnt(0)\n"
    "v_readfirstlane_b32 s59, v19\n"
    "s_cmp_eq_u32 s59, 4\n"
    "s_cbranch_scc1 .Lpolld6\n"
    "s_sleep 1\n"
    "s_sub_u32 s67, s67, 1\n"
    "s_cbranch_scc0 .Lpoll6\n"
    ".Lpolld6:\n"
    ".LG_fin:\n"
    "s_setprio 0\n"
    "s_add_u32 s94, s95, 1\n"
    "s_cmp_ge_u32 s94, 7\n"
    "s_cselect_b32 s59, 7, 0\n"
    "s_sub_u32 s94, s94, s59\n"
    "s_lshl_b32 s94, s94, 16\n"
    "s_add_u32 s94, s94, s93\n"
    "s_add_u32 m0, s89, 98304\n"
    "s_nop 0\n"
    "buffer_load_dwordx4 v28, s[48:51], s94 offen lds\n"
    "s_setprio 3\n"
    "v_max_f32 v142, 0, v142\n"
    "v_max_f32 v143, 0, v143\n"
    "v_mul_f32 v136, v136, v152\n"
    "v_mul_f32 v137, v137, v153\n"
    "v_mul_f32 v138, v138, v154\n"
    "v_mul_f32 v139, v139, v155\n"
    "v_mul_f32 v140, v140, v156\n"
    "v_mul_f32 v141, v141, v157\n"
    "s_setprio 0\n"
    "v_mfma_f32_32x32x16_bf16 v[176:191], v[116:119], v[36:39], v[176:191]\n"
    "s_add_u32 m0, m0, 0x400\n"
    "s_nop 0\n"
    "buffer_load_dwordx4 v29, s[48:51], s94 offen lds\n"
    "s_setprio 3\n"
    "v_mul_f32 v142, v142, v158\n"
    "v_mul_f32 v143, v143, v159\n"
    "v_cvt_pk_bf16_f32 v148, v136, v137\n"
    "v_cvt_pk_bf16_f32 v149, v138, v139\n"
    "v_cvt_pk_bf16_f32 v150, v140, v141\n"
    "v_cvt_pk_bf16_f32 v151, v142, v143\n"
    "buffer_store_dwordx4 v[148:151], v10, s[28:31], s62 offen offset:1024 sc1\n"
    "s_add_u32 s62, s62, 114688\n"
    "s_setprio 0\n"
    "v_mfma_f32_32x32x16_bf16 v[192:207], v[96:99], v[72:75], v[192:207]\n"
    "s_add_u32 s94, s94, 0x4000\n"
    "s_add_u32 m0, s89, 106496\n"
    "s_nop 0\n"
    "buffer_load_dwordx4 v28, s[48:51], s94 offen lds\n"
    "v_mfma_f32_32x32x16_bf16 v[208:223], v[100:103], v[72:75], v[208:223]\n"
    "s_add_u32 m0, m0, 0x400\n"
    "s_nop 0\n"
    "buffer_load_dwordx4 v29, s[48:51], s94 offen lds\n"
    "s_nop 8\n"
    "s_setprio 3\n"
    "v_max_f32 v160, 0, v160\n"
    "v_max_f32 v161, 0, v161\n"
    "v_max_f32 v162, 0, v162\n"
    "v_max_f32 v163, 0, v163\n"
    "v_max_f32 v164, 0, v164\n"
    "v_max_f32 v165, 0, v165\n"
    "v_max_f32 v166, 0, v166\n"
    "v_max_f32 v167, 0, v167\n"
    "v_mul_f32 v160, v160, v176\n"
    "s_setprio 0\n"
    "v_mfma_f32_32x32x16_bf16 v[192:207], v[104:107], v[88:91], v[192:207]\n"
    "s_add_u32 s94, s94, 0x4000\n"
    "s_add_u32 m0, s89, 114688\n"
    "s_nop 0\n"
    "buffer_load_dwordx4 v28, s[48:51], s94 offen lds\n"
    "s_setprio 3\n"
    "v_mul_f32 v161, v161, v177\n"
    "v_mul_f32 v162, v162, v178\n"
    "v_mul_f32 v163, v163, v179\n"
    "v_mul_f32 v164, v164, v180\n"
    "v_mul_f32 v165, v165, v181\n"
    "v_mul_f32 v166, v166, v182\n"
    "v_mul_f32 v167, v167, v183\n"
    "v_cvt_pk_bf16_f32 v176, v160, v161\n"
    "v_cvt_pk_bf16_f32 v177, v162, v163\n"
    "s_setprio 0\n"
    "v_mfma_f32_32x32x16_bf16 v[208:223], v[108:111], v[88:91], v[208:223]\n"
    "s_add_u32 m0, m0, 0x400\n"
    "s_nop 0\n"
    "buffer_load_dwordx4 v29, s[48:51], s94 offen lds\n"
    "s_setprio 3\n"
    "v_cvt_pk_bf16_f32 v178, v164, v165\n"
    "v_cvt_pk_bf16_f32 v179, v166, v167\n"
    "buffer_store_dwordx4 v[176:179], v10, s[28:31], s62 offen offset:0 sc1\n"
    "v_max_f32 v168, 0, v168\n"
    "v_max_f32 v169, 0, v169\n"
    "v_max_f32 v170, 0, v170\n"
    "v_max_f32 v171, 0, v171\n"
    "v_max_f32 v172, 0, v172\n"
    "v_max_f32 v173, 0, v173\n"
    "s_setprio 0\n"
    "v_mfma_f32_32x32x16_bf16 v[192:207], v[60:63], v[40:43], v[192:207]\n"
    "s_add_u32 s94, s94, 0x4000\n"
    "s_add_u32 m0, s89, 122880\n"
    "s_nop 0\n"
    "buffer_load_dwordx4 v28, s[48:51], s94 offen lds\n"
    "s_setprio 3\n"
    "v_max_f32 v174, 0, v174\n"
    "v_max_f32 v175, 0, v175\n"
    "v_mul_f32 v168, v168, v184\n"
    "v_mul_f32 v169, v169, v185\n"
    "v_mul_f32 v170, v170, v186\n"
    "v_mul_f32 v171, v171, v187\n"
    "v_mul_f32 v172, v172, v188\n"
    "v_mul_f32 v173, v173, v189\n"
    "s_setprio 0\n"
    "v_mfma_f32_32x32x16_bf16 v[208:223], v[116:119], v[40:43], v[208:223]\n"
    "s_add_u32 m0, m0, 0x400\n"
    "s_nop 0\n"
    "buffer_load_dwordx4 v29, s[48:51], s94 offen lds\n"
    "s_setprio 3\n"
    "v_mul_f32 v174, v174, v190\n"
    "v_mul_f32 v175, v175, v191\n"
    "v_cvt_pk_bf16_f32 v180, v168, v169\n"
    "v_cvt_pk_bf16_f32 v181, v170, v171\n"
    "v_cvt_pk_bf16_f32 v182, v172, v173\n"
    "v_cvt_pk_bf16_f32 v183, v174, v175\n"
    "buffer_store_dwordx4 v[180:183], v10, s[28:31], s62 offen offset:1024 sc1\n"
    "s_add_u32 s62, s62, 114688\n"
    "s_setprio 0\n"
    "v_mfma_f32_32x32x16_bf16 v[224:239], v[96:99], v[76:79], v[224:239]\n"
    "s_add_u32 s94, s95, 2\n"
    "s_cmp_ge_u32 s94, 7\n"
    "s_cselect_b32 s59, 7, 0\n"
    "s_sub_u32 s94, s94, s59\n"
    "s_lshl_b32 s94, s94, 16\n"
    "s_add_u32 s94, s94, s93\n"
    "s_add_u32 m0, s89, 131072\n"
    "s_nop 0\n"
    "buffer_load_dwordx4 v28, s[48:51], s94 offen lds\n"
    "v_mfma_f32_32x32x16_bf16 v[240:255], v[100:103], v[76:79], v[240:255]\n"
    "s_add_u32 m0, m0, 0x400\n"
    "s_nop 0\n"
    "buffer_load_dwordx4 v29, s[48:51], s94 offen lds\n"
    "s_nop 8\n"
    "s_setprio 3\n"
    "v_max_f32 v192, 0, v192\n"
    "v_max_f32 v193, 0, v193\n"
    "v_max_f32 v194, 0, v194\n"
    "v_max_f32 v195, 0, v195\n"
    "v_max_f32 v196, 0, v196\n"
    "v_max_f32 v197, 0, v197\n"
    "v_max_f32 v198, 0, v198\n"
    "v_max_f32 v199, 0, v199\n"
    "v_mul_f32 v192, v192, v208\n"
    "s_setprio 0\n"
    "v_mfma_f32_32x32x16_bf16 v[224:239], v[104:107], v[92:95], v[224:239]\n"
    "s_add_u32 s94, s94, 0x4000\n"
    "s_add_u32 m0, s89, 139264\n"
    "s_nop 0\n"
    "buffer_load_dwordx4 v28, s[48:51], s94 offen lds\n"
    "s_setprio 3\n"
    "v_mul_f32 v193, v193, v209\n"
    "v_mul_f32 v194, v194, v210\n"
    "v_mul_f32 v195, v195, v211\n"
    "v_mul_f32 v196, v196, v212\n"
    "v_mul_f32 v197, v197, v213\n"
    "v_mul_f32 v198, v198, v214\n"
    "v_mul_f32 v199, v199, v215\n"
    "v_cvt_pk_bf16_f32 v208, v192, v193\n"
    "v_cvt_pk_bf16_f32 v209, v194, v195\n"
    "s_setprio 0\n"
    "v_mfma_f32_32x32x16_bf16 v[240:255], v[108:111], v[92:95], v[240:255]\n"
    "s_add_u32 m0, m0, 0x400\n"
    "s_nop 0\n"
    "buffer_load_dwordx4 v29, s[48:51], s94 offen lds\n"
    "s_setprio 3\n"
    "v_cvt_pk_bf16_f32 v210, v196, v197\n"
    "v_cvt_pk_bf16_f32 v211, v198, v199\n"
    "buffer_store_dwordx4 v[208:211], v10, s[28:31], s62 offen offset:0 sc1\n"
    "v_max_f32 v200, 0, v200\n"
    "v_max_f32 v201, 0, v201\n"
    "v_max_f32 v202, 0, v202\n"
    "v_max_f32 v203, 0, v203\n"
    "v_max_f32 v204, 0, v204\n"
    "v_max_f32 v205, 0, v205\n"
    "s_setprio 0\n"
    "v_mfma_f32_32x32x16_bf16 v[224:239], v[60:63], v[44:47], v[224:239]\n"
    "s_add_u32 s94, s94, 0x4000\n"
    "s_add_u32 m0, s89, 147456\n"
    "s_nop 0\n"
    "buffer_load_dwordx4 v28, s[48:51], s94 offen lds\n"
    "s_setprio 3\n"
    "v_max_f32 v206, 0, v206\n"
    "v_max_f32 v207, 0, v207\n"
    "v_mul_f32 v200, v200, v216\n"
    "v_mul_f32 v201, v201, v217\n"
    "v_mul_f32 v202, v202, v218\n"
    "v_mul_f32 v203, v203, v219\n"
    "v_mul_f32 v204, v204, v220\n"
    "v_mul_f32 v205, v205, v221\n"
    "s_setprio 0\n"
    "v_mfma_f32_32x32x16_bf16 v[240:255], v[116:119], v[44:47], v[240:255]\n"
    "s_add_u32 m0, m0, 0x400\n"
    "s_nop 0\n"
    "buffer_load_dwordx4 v29, s[48:51], s94 offen lds\n"
    "s_setprio 3\n"
    "v_mul_f32 v206, v206, v222\n"
    "v_mul_f32 v207, v207, v223\n"
    "v_cvt_pk_bf16_f32 v212, v200, v201\n"
    "v_cvt_pk_bf16_f32 v213, v202, v203\n"
    "v_cvt_pk_bf16_f32 v214, v204, v205\n"
    "v_cvt_pk_bf16_f32 v215, v206, v207\n"
    "buffer_store_dwordx4 v[212:215], v10, s[28:31], s62 offen offset:1024 sc1\n"
    "s_add_u32 s62, s62, 114688\n"
    "s_setprio 0\n"
    "s_setprio 3\n"
    "s_lshl_b32 s59, s88, 7\n"
    "s_lshl_b32 s61, s53, 13\n"
    "s_add_u32 s59, s59, s61\n"
    "s_add_u32 s59, s59, 524288\n"
    "v_max_f32 v224, 0, v224\n"
    "v_max_f32 v225, 0, v225\n"
    "v_max_f32 v226, 0, v226\n"
    "v_max_f32 v227, 0, v227\n"
    "v_max_f32 v228, 0, v228\n"
    "v_max_f32 v229, 0, v229\n"
    "v_max_f32 v230, 0, v230\n"
    "v_max_f32 v231, 0, v231\n"
    "v_mul_f32 v224, v224, v240\n"
    "v_mul_f32 v225, v225, v241\n"
    "v_mul_f32 v226, v226, v242\n"
    "v_mul_f32 v227, v227, v243\n"
    "v_mul_f32 v228, v228, v244\n"
    "v_mul_f32 v229, v229, v245\n"
    "v_mul_f32 v230, v230, v246\n"
    "v_mul_f32 v231, v231, v247\n"
    "v_cvt_pk_bf16_f32 v240, v224, v225\n"
    "v_cvt_pk_bf16_f32 v241, v226, v227\n"
    "v_cvt_pk_bf16_f32 v242, v228, v229\n"
    "v_cvt_pk_bf16_f32 v243, v230, v231\n"
    "buffer_store_dwordx4 v[240:243], v10, s[28:31], s62 offen offset:0 sc1\n"
    "v_max_f32 v232, 0, v232\n"
    "v_max_f32 v233, 0, v233\n"
    "v_max_f32 v234, 0, v234\n"
    "v_max_f32 v235, 0, v235\n"
    "v_max_f32 v236, 0, v236\n"
    "v_max_f32 v237, 0, v237\n"
    "v_max_f32 v238, 0, v238\n"
    "v_max_f32 v239, 0, v239\n"
    "v_mul_f32 v232, v232, v248\n"
    "v_mul_f32 v233, v233, v249\n"
    "v_mul_f32 v234, v234, v250\n"
    "v_mul_f32 v235, v235, v251\n"
    "v_mul_f32 v236, v236, v252\n"
    "v_mul_f32 v237, v237, v253\n"
    "v_mul_f32 v238, v238, v254\n"
    "v_mul_f32 v239, v239, v255\n"
    "v_cvt_pk_bf16_f32 v244, v232, v233\n"
    "v_cvt_pk_bf16_f32 v245, v234, v235\n"
    "v_cvt_pk_bf16_f32 v246, v236, v237\n"
    "v_cvt_pk_bf16_f32 v247, v238, v239\n"
    "buffer_store_dwordx4 v[244:247], v10, s[28:31], s62 offen offset:1024 sc1\n"
    "s_add_u32 s62, s62, 114688\n"
    "ds_read_b64_tr_b16 v[128:129], v26 offset:0\n"
    "ds_read_b64_tr_b16 v[130:131], v26 offset:512\n"
    "ds_read_b64_tr_b16 v[132:133], v26 offset:1024\n"
    "ds_read_b64_tr_b16 v[134:135], v26 offset:1536\n"
    "ds_read_b64_tr_b16 v[136:137], v26 offset:8192\n"
    "ds_read_b64_tr_b16 v[138:139], v26 offset:8704\n"
    "ds_read_b64_tr_b16 v[140:141], v26 offset:9216\n"
    "ds_read_b64_tr_b16 v[142:143], v26 offset:9728\n"
    "ds_read_b64_tr_b16 v[144:145], v26 offset:16384\n"
    "ds_read_b64_tr_b16 v[146:147], v26 offset:16896\n"
    "ds_read_b64_tr_b16 v[148:149], v26 offset:17408\n"
    "ds_read_b64_tr_b16 v[150:151], v26 offset:17920\n"
    "ds_read_b64_tr_b16 v[152:153], v26 offset:24576\n"
    "ds_read_b64_tr_b16 v[154:155], v26 offset:25088\n"
    "ds_read_b64_tr_b16 v[156:157], v26 offset:25600\n"
    "ds_read_b64_tr_b16 v[158:159], v26 offset:26112\n"
    "ds_read_b64_tr_b16 v[160:161], v26 offset:32768\n"
    "ds_read_b64_tr_b16 v[162:163], v26 offset:33280\n"
    "ds_read_b64_tr_b16 v[164:165], v26 offset:33792\n"
    "ds_read_b64_tr_b16 v[166:167], v26 offset:34304\n"
    "ds_read_b64_tr_b16 v[168:169], v26 offset:40960\n"
    "ds_read_b64_tr_b16 v[170:171], v26 offset:41472\n"
    "ds_read_b64_tr_b16 v[172:173], v26 offset:41984\n"
    "ds_read_b64_tr_b16 v[174:175], v26 offset:42496\n"
    "ds_read_b64_tr_b16 v[176:177], v26 offset:49152\n"
    "ds_read_b64_tr_b16 v[178:179], v26 offset:49664\n"
    "ds_read_b64_tr_b16 v[180:181], v26 offset:50176\n"
    "ds_read_b64_tr_b16 v[182:183], v26 offset:50688\n"
    "ds_read_b64_tr_b16 v[184:185], v26 offset:57344\n"
    "ds_read_b64_tr_b16 v[186:187], v26 offset:57856\n"
    "ds_read_b64_tr_b16 v[188:189], v26 offset:58368\n"
    "ds_read_b64_tr_b16 v[190:191], v26 offset:58880\n"
    "s_waitcnt vmcnt(0)\n"
    "v_mov_b32 v16, s56\n"
    "v_mov_b32 v17, s57\n"
    "v_lshlrev_b32 v18, 3, v1\n"
    "s_mov_b64 exec, 0xffff\n"
    "buffer_store_dwordx2 v[16:17], v18, s[28:31], s59 offen sc1\n"
    "s_mov_b64 exec, -1\n"
    "s_branch .LG2\n"
    ".LG_free:\n"
    "s_lshr_b32 s59, s54, 3\n"
    "s_xor_b32 s59, s59, 0\n"
    "s_lshl_b32 s59, s59, 2\n"
    "s_add_u32 s59, s59, s52\n"
    "s_lshl_b32 s94, s59, 11\n"
    "s_lshl_b32 s59, s59, 14\n"
    "s_lshl_b32 s93, s53, 17\n"
    "s_add_u32 s93, s93, s59\n"
    "s_waitcnt lgkmcnt(0)\n"
    "s_mov_b32 s32, s4\n"
    "s_and_b32 s33, s5, 0xffff\n"
    "s_mov_b32 s34, 2097152\n"
    "s_mov_b32 s35, 0x20000\n"
    "s_cmp_ge_u32 s52, 2\n"
    "s_cselect_b32 s8, s10, s8\n"
    "s_cselect_b32 s9, s11, s9\n"
    "s_mov_b32 s40, s8\n"
    "s_and_b32 s41, s9, 0xffff\n"
    "s_mov_b32 s42, 16384\n"
    "s_mov_b32 s43, 0x20000\n"
    "s_and_b32 s90, s91, 0x400\n"
    "s_add_u32 s62, s55, 0\n"
    "s_and_b32 s62, s62, 7\n"
    "s_add_u32 m0, s91, 131072\n"
    "s_lshl_b32 s63, s62, 11\n"
    "s_add_u32 s63, s63, s90\n"
    "buffer_load_dwordx4 v10, s[40:43], s63 offen lds\n"
    "s_add_u32 m0, s94, 0\n"
    "s_lshl_b32 s62, s62, 7\n"
    "s_add_u32 s62, s62, s93\n"
    "buffer_load_dwordx4 v48, s[32:35], s62 offen lds\n"
    "s_add_u32 m0, m0, 0x400\n"
    "s_nop 0\n"
    "buffer_load_dwordx4 v49, s[32:35], s62 offen lds\n"
    "s_add_u32 s62, s55, 1\n"
    "s_and_b32 s62, s62, 7\n"
    "s_add_u32 m0, s91, 135168\n"
    "s_lshl_b32 s63, s62, 11\n"
    "s_add_u32 s63, s63, s90\n"
    "buffer_load_dwordx4 v10, s[40:43], s63 offen lds\n"
    "s_add_u32 m0, s94, 16384\n"
    "s_lshl_b32 s62, s62, 7\n"
    "s_add_u32 s62, s62, s93\n"
    "buffer_load_dwordx4 v48, s[32:35], s62 offen lds\n"
    "s_add_u32 m0, m0, 0x400\n"
    "s_nop 0\n"
    "buffer_load_dwordx4 v49, s[32:35], s62 offen lds\n"
    "s_add_u32 s62, s55, 2\n"
    "s_and_b32 s62, s62, 7\n"
    "s_add_u32 m0, s91, 139264\n"
    "s_lshl_b32 s63, s62, 11\n"
    "s_add_u32 s63, s63, s90\n"
    "buffer_load_dwordx4 v10, s[40:43], s63 offen lds\n"
    "s_add_u32 m0, s94, 32768\n"
    "s_lshl_b32 s62, s62, 7\n"
    "s_add_u32 s62, s62, s93\n"
    "buffer_load_dwordx4 v48, s[32:35], s62 offen lds\n"
    "s_add_u32 m0, m0, 0x400\n"
    "s_nop 0\n"
    "buffer_load_dwordx4 v49, s[32:35], s62 offen lds\n"
    "s_add_u32 s62, s55, 3\n"
    "s_and_b32 s62, s62, 7\n"
    "s_add_u32 m0, s91, 143360\n"
    "s_lshl_b32 s63, s62, 11\n"
    "s_add_u32 s63, s63, s90\n"
    "buffer_load_dwordx4 v10, s[40:43], s63 offen lds\n"
    "s_add_u32 m0, s94, 49152\n"
    "s_lshl_b32 s62, s62, 7\n"
    "s_add_u32 s62, s62, s93\n"
    "buffer_load_dwordx4 v48, s[32:35], s62 offen lds\n"
    "s_add_u32 m0, m0, 0x400\n"
    "s_nop 0\n"
    "v_and_b32 v6, 31, v1\n"
    "v_lshrrev_b32 v7, 5, v1\n"
    "v_bfe_u32 v8, v1, 2, 2\n"
    "v_and_b32 v9, 3, v1\n"
    "v_lshlrev_b32 v9, 3, v9\n"
    "v_bfe_u32 v11, v1, 4, 1\n"
    "v_lshl_add_u32 v9, v11, 5, v9\n"
    "v_bfe_u32 v12, v6, 1, 3\n"
    "v_xor_b32 v12, v12, v7\n"
    "v_xor_b32 v13, 0, v12\n"
    "v_lshlrev_b32 v13, 4, v13\n"
    "v_lshl_add_u32 v112, v6, 7, v13\n"
    "v_xor_b32 v13, 2, v12\n"
    "v_lshlrev_b32 v13, 4, v13\n"
    "v_lshl_add_u32 v113, v6, 7, v13\n"
    "v_xor_b32 v13, 4, v12\n"
    "v_lshlrev_b32 v13, 4, v13\n"
    "v_lshl_add_u32 v114, v6, 7, v13\n"
    "v_xor_b32 v13, 6, v12\n"
    "v_lshlrev_b32 v13, 4, v13\n"
    "v_lshl_add_u32 v115, v6, 7, v13\n"
    "v_mov_b32 v15, 0x3f803f80\n"
    "v_lshrrev_b32 v12, 2, v1\n"
    "v_and_b32 v13, 3, v1\n"
    "v_lshlrev_b32 v13, 4, v13\n"
    "v_mul_u32_u24 v12, 3584, v12\n"
    "v_add_u32 v56, v12, v13\n"
    "v_lshl_add_u32 v12, v7, 3, v8\n"
    "v_lshl_add_u32 v53, v12, 6, v9\n"
    "v_lshlrev_b32 v14, 3, v1\n"
    "s_mov_b32 s28, s22\n"
    "s_and_b32 s29, s23, 0xffff\n"
    "s_mov_b32 s30, 10485760\n"
    "s_mov_b32 s31, 0x20000\n"
    "buffer_load_dwordx2 v[4:5], off, s[28:31], 0 sc1\n"
    "s_waitcnt vmcnt(10)\n"
    "buffer_load_dwordx4 v49, s[32:35], s62 offen lds\n"
    "s_add_u32 s62, s55, 4\n"
    "s_and_b32 s62, s62, 7\n"
    "s_add_u32 m0, s91, 147456\n"
    "s_lshl_b32 s63, s62, 11\n"
    "s_add_u32 s63, s63, s90\n"
    "s_waitcnt vmcnt(10)\n"
    "buffer_load_dwordx4 v10, s[40:43], s63 offen lds\n"
    "s_barrier\n"
    "s_add_u32 m0, s94, 65536\n"
    "s_lshl_b32 s62, s62, 7\n"
    "s_add_u32 s62, s62, s93\n"
    "s_waitcnt vmcnt(10)\n"
    "buffer_load_dwordx4 v48, s[32:35], s62 offen lds\n"
    "s_add_u32 m0, m0, 0x400\n"
    "s_nop 0\n"
    "s_waitcnt vmcnt(10)\n"
    "buffer_load_dwordx4 v49, s[32:35], s62 offen lds\n"
    "s_add_u32 s62, s55, 5\n"
    "s_and_b32 s62, s62, 7\n"
    "s_add_u32 m0, s91, 151552\n"
    "s_lshl_b32 s63, s62, 11\n"
    "s_add_u32 s63, s63, s90\n"
    "s_waitcnt vmcnt(10)\n"
    "buffer_load_dwordx4 v10, s[40:43], s63 offen lds\n"
    "s_barrier\n"
    "s_add_u32 m0, s94, 81920\n"
    "s_lshl_b32 s62, s62, 7\n"
    "s_add_u32 s62, s62, s93\n"
    "s_waitcnt vmcnt(10)\n"
    "buffer_load_dwordx4 v48, s[32:35], s62 offen lds\n"
    "s_add_u32 m0, m0, 0x400\n"
    "s_nop 0\n"
    "s_waitcnt vmcnt(10)\n"
    "buffer_load_dwordx4 v49, s[32:35], s62 offen lds\n"
    "s_add_u32 s62, s55, 6\n"
    "s_and_b32 s62, s62, 7\n"
    "s_add_u32 m0, s91, 155648\n"
    "s_lshl_b32 s63, s62, 11\n"
    "s_add_u32 s63, s63, s90\n"
    "s_waitcnt vmcnt(10)\n"
    "buffer_load_dwordx4 v10, s[40:43], s63 offen lds\n"
    "s_barrier\n"
    "s_add_u32 m0, s94, 98304\n"
    "s_lshl_b32 s62, s62, 7\n"
    "s_add_u32 s62, s62, s93\n"
    "s_waitcnt vmcnt(10)\n"
    "buffer_load_dwordx4 v48, s[32:35], s62 offen lds\n"
    "s_add_u32 m0, m0, 0x400\n"
    "s_nop 0\n"
    "s_waitcnt vmcnt(10)\n"
    "buffer_load_dwordx4 v49, s[32:35], s62 offen lds\n"
    "s_add_u32 s62, s55, 7\n"
    "s_and_b32 s62, s62, 7\n"
    "s_add_u32 m0, s91, 159744\n"
    "s_lshl_b32 s63, s62, 11\n"
    "s_add_u32 s63, s63, s90\n"
    "s_waitcnt vmcnt(10)\n"
    "buffer_load_dwordx4 v10, s[40:43], s63 offen lds\n"
    "s_add_u32 m0, s94, 114688\n"
    "s_lshl_b32 s62, s62, 7\n"
    "s_add_u32 s62, s62, s93\n"
    "s_waitcnt vmcnt(10)\n"
    "buffer_load_dwordx4 v48, s[32:35], s62 offen lds\n"
    "s_barrier\n"
    "s_add_u32 m0, m0, 0x400\n"
    "s_nop 0\n"
    "s_waitcnt vmcnt(10)\n"
    "buffer_load_dwordx4 v49, s[32:35], s62 offen lds\n"
    "s_waitcnt vmcnt(9)\n"
    "s_barrier\n"
    "s_waitcnt vmcnt(6)\n"
    "s_barrier\n"
    "s_waitcnt vmcnt(3)\n"
    "s_barrier\n"
    "s_waitcnt vmcnt(0)\n"
    "s_barrier\n"
    "v_readfirstlane_b32 s56, v4\n"
    "v_readfirstlane_b32 s57, v5\n"
    "s_add_u32 s56, s56, 0x7f4a7c15\n"
    "s_addc_u32 s57, s57, 0x9e3779b9\n"
    "s_lshr_b32 s59, s54, 3\n"
    "s_xor_b32 s59, s59, 1\n"
    "s_lshl_b32 s59, s59, 2\n"
    "s_add_u32 s59, s59, s52\n"
    "s_lshl_b32 s94, s59, 11\n"
    "s_lshl_b32 s59, s59, 14\n"
    "s_lshl_b32 s93, s53, 17\n"
    "s_add_u32 s93, s93, s59\n"
    "s_lshl_b32 s90, s52, 14\n"
    "s_lshl_b32 s59, s54, 6\n"
    "s_add_u32 s90, s90, s59\n"
    "s_lshr_b32 s86, s52, 1\n"
    "s_mul_i32 s86, s86, 6144\n"
    "s_and_b32 s59, s52, 1\n"
    "s_lshl_b32 s59, s59, 10\n"
    "s_add_u32 s86, s86, s59\n"
    "s_mov_b32 s36, s6\n"
    "s_and_b32 s37, s7, 0xffff\n"
    "s_mov_b32 s38, 524288\n"
    "s_mov_b32 s39, 0x20000\n"
    "s_barrier\n"
    "s_barrier\n"
    "s_add_u32 s62, s55, 0\n"
    "s_and_b32 s62, s62, 7\n"
    "s_add_u32 m0, s91, 131072\n"
    "s_lshl_b32 s63, s62, 16\n"
    "s_add_u32 s63, s63, s90\n"
    "buffer_load_dwordx4 v54, s[36:39], s63 offen lds\n"
    "s_add_u32 m0, s94, 0\n"
    "s_lshl_b32 s62, s62, 7\n"
    "s_add_u32 s62, s62, s93\n"
    "buffer_load_dwordx4 v48, s[32:35], s62 offen lds\n"
    "s_add_u32 m0, m0, 0x400\n"
    "s_nop 0\n"
    "buffer_load_dwordx4 v49, s[32:35], s62 offen lds\n"
    "s_add_u32 s62, s55, 1\n"
    "s_and_b32 s62, s62, 7\n"
    "s_add_u32 m0, s91, 137216\n"
    "s_lshl_b32 s63, s62, 16\n"
    "s_add_u32 s63, s63, s90\n"
    "buffer_load_dwordx4 v54, s[36:39], s63 offen lds\n"
    "s_add_u32 m0, s94, 16384\n"
    "s_lshl_b32 s62, s62, 7\n"
    "s_add_u32 s62, s62, s93\n"
    "buffer_load_dwordx4 v48, s[32:35], s62 offen lds\n"
    "s_add_u32 m0, m0, 0x400\n"
    "s_nop 0\n"
    "buffer_load_dwordx4 v49, s[32:35], s62 offen lds\n"
    "s_add_u32 s62, s55, 2\n"
    "s_and_b32 s62, s62, 7\n"
    "s_add_u32 m0, s91, 143360\n"
    "s_lshl_b32 s63, s62, 16\n"
    "s_add_u32 s63, s63, s90\n"
    "buffer_load_dwordx4 v54, s[36:39], s63 offen lds\n"
    "s_add_u32 m0, s94, 32768\n"
    "s_lshl_b32 s62, s62, 7\n"
    "s_add_u32 s62, s62, s93\n"
    "buffer_load_dwordx4 v48, s[32:35], s62 offen lds\n"
    "s_add_u32 m0, m0, 0x400\n"
    "s_nop 0\n"
    "s_waitcnt vmcnt(7)\n"
    "buffer_load_dwordx4 v49, s[32:35], s62 offen lds\n"
    "s_add_u32 s62, s55, 3\n"
    "s_and_b32 s62, s62, 7\n"
    "s_add_u32 m0, s91, 149504\n"
    "s_lshl_b32 s63, s62, 16\n"
    "s_add_u32 s63, s63, s90\n"
    "s_waitcnt vmcnt(7)\n"
    "buffer_load_dwordx4 v54, s[36:39], s63 offen lds\n"
    "s_add_u32 m0, s94, 49152\n"
    "s_lshl_b32 s62, s62, 7\n"
    "s_add_u32 s62, s62, s93\n"
    "s_waitcnt vmcnt(7)\n"
    "buffer_load_dwordx4 v48, s[32:35], s62 offen lds\n"
    "s_barrier\n"
    "s_add_u32 m0, m0, 0x400\n"
    "s_nop 0\n"
    "s_waitcnt vmcnt(7)\n"
    "buffer_load_dwordx4 v49, s[32:35], s62 offen lds\n"
    "s_add_u32 s62, s55, 4\n"
    "s_and_b32 s62, s62, 7\n"
    "s_add_u32 m0, s86, 135168\n"
    "s_lshl_b32 s63, s62, 16\n"
    "s_add_u32 s63, s63, s90\n"
    "s_waitcnt vmcnt(7)\n"
    "buffer_load_dwordx4 v54, s[36:39], s63 offen lds\n"
    "s_add_u32 m0, s94, 65536\n"
    "s_lshl_b32 s62, s62, 7\n"
    "s_add_u32 s62, s62, s93\n"
    "s_waitcnt vmcnt(7)\n"
    "buffer_load_dwordx4 v48, s[32:35], s62 offen lds\n"
    "s_barrier\n"
    "s_add_u32 m0, m0, 0x400\n"
    "s_nop 0\n"
    "s_waitcnt vmcnt(7)\n"
    "buffer_load_dwordx4 v49, s[32:35], s62 offen lds\n"
    "s_add_u32 s62, s55, 5\n"
    "s_and_b32 s62, s62, 7\n"
    "s_add_u32 m0, s86, 147456\n"
    "s_lshl_b32 s63, s62, 16\n"
    "s_add_u32 s63, s63, s90\n"
    "s_waitcnt vmcnt(7)\n"
    "buffer_load_dwordx4 v54, s[36:39], s63 offen lds\n"
    "s_add_u32 m0, s94, 81920\n"
    "s_lshl_b32 s62, s62, 7\n"
    "s_add_u32 s62, s62, s93\n"
    "s_waitcnt vmcnt(7)\n"
    "buffer_load_dwordx4 v48, s[32:35], s62 offen lds\n"
    "s_barrier\n"
    "s_add_u32 m0, m0, 0x400\n"
    "s_nop 0\n"
    "s_waitcnt vmcnt(7)\n"
    "buffer_load_dwordx4 v49, s[32:35], s62 offen lds\n"
    "s_add_u32 s62, s55, 6\n"
    "s_and_b32 s62, s62, 7\n"
    "s_add_u32 m0, s91, 155648\n"
    "s_lshl_b32 s63, s62, 16\n"
    "s_add_u32 s63, s63, s90\n"
    "s_waitcnt vmcnt(7)\n"
    "buffer_load_dwordx4 v54, s[36:39], s63 offen lds\n"
    "s_add_u32 m0, s94, 98304\n"
    "s_lshl_b32 s62, s62, 7\n"
    "s_add_u32 s62, s62, s93\n"
    "s_waitcnt vmcnt(7)\n"
    "buffer_load_dwordx4 v48, s[32:35], s62 offen lds\n"
    "s_barrier\n"
    "s_add_u32 m0, m0, 0x400\n"
    "s_nop 0\n"
    "s_waitcnt vmcnt(7)\n"
    "buffer_load_dwordx4 v49, s[32:35], s62 offen lds\n"
    "s_add_u32 s62, s55, 7\n"
    "s_and_b32 s62, s62, 7\n"
    "s_add_u32 m0, s91, 159744\n"
    "s_lshl_b32 s63, s62, 16\n"
    "s_add_u32 s63, s63, s90\n"
    "s_waitcnt vmcnt(7)\n"
    "buffer_load_dwordx4 v54, s[36:39], s63 offen lds\n"
    "s_add_u32 m0, s94, 114688\n"
    "s_lshl_b32 s62, s62, 7\n"
    "s_add_u32 s62, s62, s93\n"
    "s_waitcnt vmcnt(7)\n"
    "buffer_load_dwordx4 v48, s[32:35], s62 offen lds\n"
    "s_barrier\n"
    "s_add_u32 m0, m0, 0x400\n"
    "s_nop 0\n"
    "s_waitcnt vmcnt(7)\n"
    "buffer_load_dwordx4 v49, s[32:35], s62 offen lds\n"
    "s_waitcnt vmcnt(6)\n"
    "s_barrier\n"
    "s_waitcnt vmcnt(3)\n"
    "s_barrier\n"
    "s_waitcnt vmcnt(0)\n"
    "s_barrier\n"
    "v_mov_b32 v12, 156672\n"
    "v_mov_b32 v16, 0\n"
    "v_mov_b32 v17, 0\n"
    "ds_write_b64 v12, v[16:17]\n"
    "ds_write_b64 v12, v[16:17] offset:8\n"
    "ds_write_b64 v12, v[16:17] offset:16\n"
    "ds_write_b64 v12, v[16:17] offset:32\n"
    "ds_write_b64 v12, v[16:17] offset:40\n"
    "s_mov_b32 s36, s12\n"
    "s_and_b32 s37, s13, 0xffff\n"
    "s_mov_b32 s38, 524288\n"
    "s_mov_b32 s39, 0x20000\n"
    "s_mov_b32 s48, s18\n"
    "s_and_b32 s49, s19, 0xffff\n"
    "s_mov_b32 s50, 1835008\n"
    "s_mov_b32 s51, 0x20000\n"
    "s_lshr_b32 s59, s54, 1\n"
    "s_lshl_b32 s59, s59, 7\n"
    "s_lshr_b32 s60, s52, 1\n"
    "s_lshl_b32 s60, s60, 6\n"
    "s_add_u32 s59, s59, s60\n"
    "v_add_u32 v28, s59, v54\n"
    "v_xor_b32 v29, 64, v28\n"
    "s_lshl_b32 s89, s52, 11\n"
    "v_lshl_add_u32 v12, v7, 2, v8\n"
    "v_lshl_add_u32 v12, v12, 6, v9\n"
    "v_add_u32 v26, s89, v12\n"
    "v_add_u32 v25, 0x10000, v26\n"
    "v_add_u32 v22, 0x20000, v26\n"
    "s_and_b32 s61, s54, 1\n"
    "s_lshl_b32 s61, s61, 1\n"
    "s_and_b32 s60, s52, 1\n"
    "s_add_u32 s61, s61, s60\n"
    "s_lshl_b32 s60, s53, 2\n"
    "s_add_u32 s92, s61, s60\n"
    "s_andn2_b32 s91, s92, 1\n"
    "s_lshl_b32 s91, s91, 1\n"
    "s_add_u32 s91, s91, s52\n"
    "s_lshl_b32 s91, s91, 14\n"
    "s_andn2_b32 s59, s59, 64\n"
    "s_add_u32 s91, s91, s59\n"
    "s_add_u32 s74, s91, 0x2000\n"
    "v_lshrrev_b32 v12, 3, v1\n"
    "v_and_b32 v27, 0x70, v10\n"
    "v_lshl_add_u32 v27, v12, 10, v27\n"
    "v_lshrrev_b32 v11, 2, v1\n"
    "v_min_u32 v11, 13, v11\n"
    "v_lshlrev_b32 v11, 7, v11\n"
    "s_lshr_b32 s59, s54, 1\n"
    "s_sub_u32 s59, s59, s55\n"
    "s_and_b32 s59, s59, 7\n"
    "s_lshl_b32 s59, s59, 14\n"
    "s_lshl_b32 s61, s61, 12\n"
    "s_add_u32 s59, s59, s61\n"
    "s_lshr_b32 s60, s52, 1\n"
    "s_lshl_b32 s60, s60, 2\n"
    "v_bfe_u32 v12, v6, 1, 3\n"
    "v_xor_b32 v12, s60, v12\n"
    "v_lshlrev_b32 v12, 4, v12\n"
    "v_lshl_add_u32 v12, v6, 7, v12\n"
    "v_lshl_add_u32 v12, v7, 3, v12\n"
    "v_add_u32 v12, s59, v12\n"
    "v_xor_b32 v13, 0, v12\n"
    "ds_read_b64 v[120:121], v13\n"
    "v_xor_b32 v13, 16, v12\n"
    "ds_read_b64 v[122:123], v13\n"
    "v_xor_b32 v13, 32, v12\n"
    "ds_read_b64 v[124:125], v13\n"
    "v_xor_b32 v13, 48, v12\n"
    "ds_read_b64 v[126:127], v13\n"
    "s_xor_b32 s60, s92, 1\n"
    "s_mul_i32 s61, s52, 28672\n"
    "s_add_u32 s61, s61, 3145728\n"
    "s_mul_i32 s59, s92, 114688\n"
    "s_add_u32 s59, s59, s61\n"
    "v_add_u32 v24, s59, v10\n"
    "s_mul_i32 s59, s60, 114688\n"
    "s_add_u32 s59, s59, s61\n"
    "v_add_u32 v23, s59, v10\n"
    "s_and_b32 s59, s53, 3\n"
    "s_lshl_b32 s59, s59, 1\n"
    "s_and_b32 s60, s54, 7\n"
    "s_add_u32 s95, s59, s60\n"
    "s_cmp_ge_u32 s95, 7\n"
    "s_cselect_b32 s59, 7, 0\n"
    "s_sub_u32 s95, s95, s59\n"
    "s_mul_i32 s93, s52, 458752\n"
    "v_mov_b32 v20, 156672\n"
    "v_mov_b32 v21, 1\n"
    "s_waitcnt lgkmcnt(0)\n"
    "s_barrier\n"
    "v_lshlrev_b32 v12, 2, v6\n"
    "v_add_u32 v12, 155648, v12\n"
    "ds_read_b32 v32, v12 offset:0\n"
    "ds_read_b32 v36, v12 offset:128\n"
    "ds_read_b32 v40, v12 offset:256\n"
    "ds_read_b32 v44, v12 offset:384\n"
    "s_and_b32 s59, s54, 1\n"
    "s_lshl_b32 s59, s59, 1\n"
    "s_and_b32 s60, s52, 1\n"
    "s_add_u32 s59, s59, s60\n"
    "s_lshl_b32 s59, s59, 7\n"
    "v_add_u32 v12, s59, v12\n"
    "ds_read_b32 v31, v12 offset:512\n"
    "s_cmp_lg_u32 s52, 0\n"
    "s_cbranch_scc1 .Lsf7\n"
    "s_lshl_b32 s59, s53, 4\n"
    "s_add_u32 s59, s59, s54\n"
    "s_lshl_b32 s59, s59, 7\n"
    "s_add_u32 s59, s59, 32768\n"
    "v_mov_b32 v16, s56\n"
    "v_mov_b32 v17, s57\n"
    "v_lshlrev_b32 v18, 3, v1\n"
    "s_mov_b64 exec, 0xffff\n"
    "buffer_store_dwordx2 v[16:17], v18, s[28:31], s59 offen sc1\n"
    "s_mov_b64 exec, -1\n"
    ".Lsf7:\n"
    "s_waitcnt lgkmcnt(0)\n"
    "s_barrier\n"
    "s_setprio 3\n"
    "s_lshl_b32 s59, s53, 1\n"
    "s_lshr_b32 s61, s54, 3\n"
    "s_add_u32 s59, s59, s61\n"
    "s_lshl_b32 s59, s59, 2\n"
    "s_add_u32 s88, s59, s52\n"
    "s_cmp_ge_u32 s88, 112\n"
    "s_cbranch_scc1 .LG_nocs\n"
    "s_bitcmp1_b32 s88, 0\n"
    "s_cselect_b32 s14, s16, s14\n"
    "s_cselect_b32 s15, s17, s15\n"
    "s_mov_b32 s40, s14\n"
    "s_and_b32 s41, s15, 0xffff\n"
    "s_mov_b32 s42, 1835008\n"
    "s_mov_b32 s43, 0x20000\n"
    "s_lshr_b32 s86, s88, 1\n"
    "s_lshl_b32 s86, s86, 6\n"
    "s_lshl_b32 s61, s52, 15\n"
    "s_mov_b32 m0, s61\n"
    "s_mov_b32 s63, 32\n"
    ".LG_csl:\n"
    "s_waitcnt vmcnt(3)\n"
    "buffer_load_dwordx4 v56, s[40:43], s86 offen lds\n"
    "s_add_u32 m0, m0, 0x400\n"
    "s_add_u32 s86, s86, 57344\n"
    "s_sub_u32 s63, s63, 1\n"
    "s_cmp_lg_u32 s63, 0\n"
    "s_cbranch_scc1 .LG_csl\n"
    "v_add_u32 v53, s61, v53\n"
    "v_mov_b32 v16, v15\n"
    "v_mov_b32 v17, v15\n"
    "v_mov_b32 v18, v15\n"
    "v_mov_b32 v19, v15\n"
    "s_waitcnt vmcnt(0)\n"
    "s_setprio 0\n"
    "ds_read_b64_tr_b16 v[64:65], v53 offset:0\n"
    "ds_read_b64_tr_b16 v[66:67], v53 offset:256\n"
    "ds_read_b64_tr_b16 v[68:69], v53 offset:1024\n"
    "ds_read_b64_tr_b16 v[70:71], v53 offset:1280\n"
    "ds_read_b64_tr_b16 v[72:73], v53 offset:2048\n"
    "ds_read_b64_tr_b16 v[74:75], v53 offset:2304\n"
    "ds_read_b64_tr_b16 v[76:77], v53 offset:3072\n"
    "ds_read_b64_tr_b16 v[78:79], v53 offset:3328\n"
    "ds_read_b64_tr_b16 v[80:81], v53 offset:4096\n"
    "ds_read_b64_tr_b16 v[82:83], v53 offset:4352\n"
    "ds_read_b64_tr_b16 v[84:85], v53 offset:5120\n"
    "ds_read_b64_tr_b16 v[86:87], v53 offset:5376\n"
    "ds_read_b64_tr_b16 v[88:89], v53 offset:6144\n"
    "ds_read_b64_tr_b16 v[90:91], v53 offset:6400\n"
    "ds_read_b64_tr_b16 v[92:93], v53 offset:7168\n"
    "ds_read_b64_tr_b16 v[94:95], v53 offset:7424\n"
    "s_waitcnt lgkmcnt(0)\n"
    "v_mfma_f32_32x32x16_bf16 v[128:143], v[16:19], v[64:67], 0\n"
    "v_mfma_f32_32x32x16_bf16 v[128:143], v[16:19], v[68:71], v[128:143]\n"
    "v_mfma_f32_32x32x16_bf16 v[128:143], v[16:19], v[72:75], v[128:143]\n"
    "v_mfma_f32_32x32x16_bf16 v[128:143], v[16:19], v[76:79], v[128:143]\n"
    "v_mfma_f32_32x32x16_bf16 v[128:143], v[16:19], v[80:83], v[128:143]\n"
    "v_mfma_f32_32x32x16_bf16 v[128:143], v[16:19], v[84:87], v[128:143]\n"
    "v_mfma_f32_32x32x16_bf16 v[128:143], v[16:19], v[88:91], v[128:143]\n"
    "v_mfma_f32_32x32x16_bf16 v[128:143], v[16:19], v[92:95], v[128:143]\n"
    "ds_read_b64_tr_b16 v[64:65], v53 offset:8192\n"
    "ds_read_b64_tr_b16 v[66:67], v53 offset:8448\n"
    "ds_read_b64_tr_b16 v[68:69], v53 offset:9216\n"
    "ds_read_b64_tr_b16 v[70:71], v53 offset:9472\n"
    "ds_read_b64_tr_b16 v[72:73], v53 offset:10240\n"
    "ds_read_b64_tr_b16 v[74:75], v53 offset:10496\n"
    "ds_read_b64_tr_b16 v[76:77], v53 offset:11264\n"
    "ds_read_b64_tr_b16 v[78:79], v53 offset:11520\n"
    "ds_read_b64_tr_b16 v[80:81], v53 offset:12288\n"
    "ds_read_b64_tr_b16 v[82:83], v53 offset:12544\n"
    "ds_read_b64_tr_b16 v[84:85], v53 offset:13312\n"
    "ds_read_b64_tr_b16 v[86:87], v53 offset:13568\n"
    "ds_read_b64_tr_b16 v[88:89], v53 offset:14336\n"
    "ds_read_b64_tr_b16 v[90:91], v53 offset:14592\n"
    "ds_read_b64_tr_b16 v[92:93], v53 offset:15360\n"
    "ds_read_b64_tr_b16 v[94:95], v53 offset:15616\n"
    "s_waitcnt lgkmcnt(0)\n"
    "v_mfma_f32_32x32x16_bf16 v[128:143], v[16:19], v[64:67], v[128:143]\n"
    "v_mfma_f32_32x32x16_bf16 v[128:143], v[16:19], v[68:71], v[128:143]\n"
    "v_mfma_f32_32x32x16_bf16 v[128:143], v[16:19], v[72:75], v[128:143]\n"
    "v_mfma_f32_32x32x16_bf16 v[128:143], v[16:19], v[76:79], v[128:143]\n"
    "v_mfma_f32_32x32x16_bf16 v[128:143], v[16:19], v[80:83], v[128:143]\n"
    "v_mfma_f32_32x32x16_bf16 v[128:143], v[16:19], v[84:87], v[128:143]\n"
    "v_mfma_f32_32x32x16_bf16 v[128:143], v[16:19], v[88:91], v[128:143]\n"
    "v_mfma_f32_32x32x16_bf16 v[128:143], v[16:19], v[92:95], v[128:143]\n"
    "ds_read_b64_tr_b16 v[64:65], v53 offset:16384\n"
    "ds_read_b64_tr_b16 v[66:67], v53 offset:16640\n"
    "ds_read_b64_tr_b16 v[68:69], v53 offset:17408\n"
    "ds_read_b64_tr_b16 v[70:71], v53 offset:17664\n"
    "ds_read_b64_tr_b16 v[72:73], v53 offset:18432\n"
    "ds_read_b64_tr_b16 v[74:75], v53 offset:18688\n"
    "ds_read_b64_tr_b16 v[76:77], v53 offset:19456\n"
    "ds_read_b64_tr_b16 v[78:79], v53 offset:19712\n"
    "ds_read_b64_tr_b16 v[80:81], v53 offset:20480\n"
    "ds_read_b64_tr_b16 v[82:83], v53 offset:20736\n"
    "ds_read_b64_tr_b16 v[84:85], v53 offset:21504\n"
    "ds_read_b64_tr_b16 v[86:87], v53 offset:21760\n"
    "ds_read_b64_tr_b16 v[88:89], v53 offset:22528\n"
    "ds_read_b64_tr_b16 v[90:91], v53 offset:22784\n"
    "ds_read_b64_tr_b16 v[92:93], v53 offset:23552\n"
    "ds_read_b64_tr_b16 v[94:95], v53 offset:23808\n"
    "s_waitcnt lgkmcnt(0)\n"
    "v_mfma_f32_32x32x16_bf16 v[128:143], v[16:19], v[64:67], v[128:143]\n"
    "v_mfma_f32_32x32x16_bf16 v[128:143], v[16:19], v[68:71], v[128:143]\n"
    "v_mfma_f32_32x32x16_bf16 v[128:143], v[16:19], v[72:75], v[128:143]\n"
    "v_mfma_f32_32x32x16_bf16 v[128:143], v[16:19], v[76:79], v[128:143]\n"
    "v_mfma_f32_32x32x16_bf16 v[128:143], v[16:19], v[80:83], v[128:143]\n"
    "v_mfma_f32_32x32x16_bf16 v[128:143], v[16:19], v[84:87], v[128:143]\n"
    "v_mfma_f32_32x32x16_bf16 v[128:143], v[16:19], v[88:91], v[128:143]\n"
    "v_mfma_f32_32x32x16_bf16 v[128:143], v[16:19], v[92:95], v[128:143]\n"
    "ds_read_b64_tr_b16 v[64:65], v53 offset:24576\n"
    "ds_read_b64_tr_b16 v[66:67], v53 offset:24832\n"
    "ds_read_b64_tr_b16 v[68:69], v53 offset:25600\n"
    "ds_read_b64_tr_b16 v[70:71], v53 offset:25856\n"
    "ds_read_b64_tr_b16 v[72:73], v53 offset:26624\n"
    "ds_read_b64_tr_b16 v[74:75], v53 offset:26880\n"
    "ds_read_b64_tr_b16 v[76:77], v53 offset:27648\n"
    "ds_read_b64_tr_b16 v[78:79], v53 offset:27904\n"
    "ds_read_b64_tr_b16 v[80:81], v53 offset:28672\n"
    "ds_read_b64_tr_b16 v[82:83], v53 offset:28928\n"
    "ds_read_b64_tr_b16 v[84:85], v53 offset:29696\n"
    "ds_read_b64_tr_b16 v[86:87], v53 offset:29952\n"
    "ds_read_b64_tr_b16 v[88:89], v53 offset:30720\n"
    "ds_read_b64_tr_b16 v[90:91], v53 offset:30976\n"
    "ds_read_b64_tr_b16 v[92:93], v53 offset:31744\n"
    "ds_read_b64_tr_b16 v[94:95], v53 offset:32000\n"
    "s_waitcnt lgkmcnt(0)\n"
    "v_mfma_f32_32x32x16_bf16 v[128:143], v[16:19], v[64:67], v[128:143]\n"
    "v_mfma_f32_32x32x16_bf16 v[128:143], v[16:19], v[68:71], v[128:143]\n"
    "v_mfma_f32_32x32x16_bf16 v[128:143], v[16:19], v[72:75], v[128:143]\n"
    "v_mfma_f32_32x32x16_bf16 v[128:143], v[16:19], v[76:79], v[128:143]\n"
    "v_mfma_f32_32x32x16_bf16 v[128:143], v[16:19], v[80:83], v[128:143]\n"
    "v_mfma_f32_32x32x16_bf16 v[128:143], v[16:19], v[84:87], v[128:143]\n"
    "v_mfma_f32_32x32x16_bf16 v[128:143], v[16:19], v[88:91], v[128:143]\n"
    "v_mfma_f32_32x32x16_bf16 v[128:143], v[16:19], v[92:95], v[128:143]\n"
    "s_setprio 3\n"
    "s_nop 11\n"
    "v_mul_f32 v17, 0.5, v128\n"
    "v_cvt_pk_bf16_f32 v16, v17, v17\n"
    "v_lshlrev_b32 v16, 16, v16\n"
    "v_sub_f32 v16, v17, v16\n"
    "v_cvt_pk_bf16_f32 v16, v17, v16\n"
    "v_mov_b32 v17, s56\n"
    "s_lshl_b32 s59, s88, 9\n"
    "s_add_u32 s59, s59, 786432\n"
    "buffer_store_dwordx2 v[16:17], v14, s[28:31], s59 offen sc1\n"
    "s_branch .LG_cs9\n"
    ".LG_nocs:\n"
    "s_waitcnt vmcnt(1)\n"
    "s_waitcnt vmcnt(1)\n"
    "s_waitcnt vmcnt(1)\n"
    "s_waitcnt vmcnt(1)\n"
    ".LG_cs9:\n"
    "s_waitcnt vmcnt(1)\n"
    "s_waitcnt vmcnt(1)\n"
    "s_waitcnt vmcnt(1)\n"
    "s_waitcnt vmcnt(0)\n"
    "s_mov_b64 exec, 1\n"
    "ds_add_u32 v20, v21 offset:0\n"
    "s_mov_b64 exec, -1\n"
    "s_cmp_lg_u32 s88, 127\n"
    "s_cbranch_scc1 .LG_noep\n"
    "v_lshlrev_b32 v12, 7, v1\n"
    "s_mov_b32 s60, 32768\n"
    "s_mov_b32 s67, 100000\n"
    ".Lpoll8:\n"
    "buffer_load_dwordx2 v[16:17], v12, s[28:31], s60 offen sc1\n"
    "s_waitcnt vmcnt(0)\n"
    "v_cmp_ne_u64 vcc, s[56:57], v[16:17]\n"
    "s_nop 1\n"
    "s_cmp_eq_u64 vcc, 0\n"
    "s_cbranch_scc1 .Lpolld8\n"
    "s_sleep 8\n"
    "s_sub_u32 s67, s67, 1\n"
    "s_cbranch_scc0 .Lpoll8\n"
    ".Lpolld8:\n"
    "s_mov_b32 s60, 40960\n"
    "s_mov_b32 s67, 100000\n"
    ".Lpoll9:\n"
    "buffer_load_dwordx2 v[16:17], v12, s[28:31], s60 offen sc1\n"
    "s_waitcnt vmcnt(0)\n"
    "v_cmp_ne_u64 vcc, s[56:57], v[16:17]\n"
    "s_nop 1\n"
    "s_cmp_eq_u64 vcc, 0\n"
    "s_cbranch_scc1 .Lpolld9\n"
    "s_sleep 8\n"
    "s_sub_u32 s67, s67, 1\n"
    "s_cbranch_scc0 .Lpoll9\n"
    ".Lpolld9:\n"
    "s_mov_b32 s60, 49152\n"
    "s_mov_b32 s67, 100000\n"
    ".Lpoll10:\n"
    "buffer_load_dwordx2 v[16:17], v12, s[28:31], s60 offen sc1\n"
    "s_waitcnt vmcnt(0)\n"
    "v_cmp_ne_u64 vcc, s[56:57], v[16:17]\n"
    "s_nop 1\n"
    "s_cmp_eq_u64 vcc, 0\n"
    "s_cbranch_scc1 .Lpolld10\n"
    "s_sleep 8\n"
    "s_sub_u32 s67, s67, 1\n"
    "s_cbranch_scc0 .Lpoll10\n"
    ".Lpolld10:\n"
    "s_mov_b32 s60, 57344\n"
    "s_mov_b32 s67, 100000\n"
    ".Lpoll11:\n"
    "buffer_load_dwordx2 v[16:17], v12, s[28:31], s60 offen sc1\n"
    "s_waitcnt vmcnt(0)\n"
    "v_cmp_ne_u64 vcc, s[56:57], v[16:17]\n"
    "s_nop 1\n"
    "s_cmp_eq_u64 vcc, 0\n"
    "s_cbranch_scc1 .Lpolld11\n"
    "s_sleep 8\n"
    "s_sub_u32 s67, s67, 1\n"
    "s_cbranch_scc0 .Lpoll11\n"
    ".Lpolld11:\n"
    "s_mov_b32 s60, 0\n"
    "v_mov_b32 v16, s56\n"
    "v_mov_b32 v17, s57\n"
    "v_lshlrev_b32 v18, 3, v1\n"
    "s_mov_b64 exec, 0xffff\n"
    "buffer_store_dwordx2 v[16:17], v18, s[28:31], s60 offen sc1\n"
    "s_mov_b64 exec, -1\n"
    ".LG_noep:\n"
    "s_mov_b32 s67, 400000\n"
    ".Lpoll12:\n"
    "ds_read_b32 v13, v20 offset:0\n"
    "s_waitcnt lgkmcnt(0)\n"
    "v_readfirstlane_b32 s59, v13\n"
    "s_cmp_eq_u32 s59, 4\n"
    "s_cbranch_scc1 .Lpolld12\n"
    "s_sleep 1\n"
    "s_sub_u32 s67, s67, 1\n"
    "s_cbranch_scc0 .Lpoll12\n"
    ".Lpolld12:\n"
    "s_add_u32 s94, s95, 5\n"
    "s_cmp_ge_u32 s94, 7\n"
    "s_cselect_b32 s59, 7, 0\n"
    "s_sub_u32 s94, s94, s59\n"
    "s_lshl_b32 s94, s94, 16\n"
    "s_add_u32 s94, s94, s93\n"
    "s_add_u32 m0, s89, 0\n"
    "s_nop 0\n"
    "buffer_load_dwordx4 v28, s[48:51], s94 offen lds\n"
    "s_add_u32 m0, m0, 0x400\n"
    "s_nop 0\n"
    "buffer_load_dwordx4 v29, s[48:51], s94 offen lds\n"
    "s_add_u32 s94, s94, 0x4000\n"
    "s_add_u32 m0, s89, 8192\n"
    "s_nop 0\n"
    "buffer_load_dwordx4 v28, s[48:51], s94 offen lds\n"
    "s_add_u32 m0, m0, 0x400\n"
    "s_nop 0\n"
    "buffer_load_dwordx4 v29, s[48:51], s94 offen lds\n"
    "s_add_u32 s94, s94, 0x4000\n"
    "s_add_u32 m0, s89, 16384\n"
    "s_waitcnt vmcnt(3)\n"
    "buffer_load_dwordx4 v28, s[48:51], s94 offen lds\n"
    "s_add_u32 m0, m0, 0x400\n"
    "s_waitcnt vmcnt(3)\n"
    "buffer_load_dwordx4 v29, s[48:51], s94 offen lds\n"
    "s_add_u32 s94, s94, 0x4000\n"
    "s_add_u32 m0, s89, 24576\n"
    "s_waitcnt vmcnt(3)\n"
    "buffer_load_dwordx4 v28, s[48:51], s94 offen lds\n"
    "s_add_u32 m0, m0, 0x400\n"
    "s_waitcnt vmcnt(3)\n"
    "buffer_load_dwordx4 v29, s[48:51], s94 offen lds\n"
    "s_add_u32 s94, s95, 6\n"
    "s_cmp_ge_u32 s94, 7\n"
    "s_cselect_b32 s59, 7, 0\n"
    "s_sub_u32 s94, s94, s59\n"
    "s_lshl_b32 s94, s94, 16\n"
    "s_add_u32 s94, s94, s93\n"
    "s_add_u32 m0, s89, 32768\n"
    "s_waitcnt vmcnt(3)\n"
    "buffer_load_dwordx4 v28, s[48:51], s94 offen lds\n"
    "s_add_u32 m0, m0, 0x400\n"
    "s_waitcnt vmcnt(3)\n"
    "buffer_load_dwordx4 v29, s[48:51], s94 offen lds\n"
    "s_add_u32 s94, s94, 0x4000\n"
    "s_add_u32 m0, s89, 40960\n"
    "s_waitcnt vmcnt(3)\n"
    "buffer_load_dwordx4 v28, s[48:51], s94 offen lds\n"
    "s_add_u32 m0, m0, 0x400\n"
    "s_waitcnt vmcnt(3)\n"
    "buffer_load_dwordx4 v29, s[48:51], s94 offen lds\n"
    "s_add_u32 s94, s94, 0x4000\n"
    "s_add_u32 m0, s89, 49152\n"
    "s_waitcnt vmcnt(3)\n"
    "buffer_load_dwordx4 v28, s[48:51], s94 offen lds\n"
    "s_add_u32 m0, m0, 0x400\n"
    "s_waitcnt vmcnt(3)\n"
    "buffer_load_dwordx4 v29, s[48:51], s94 offen lds\n"
    "s_add_u32 s94, s94, 0x4000\n"
    "s_add_u32 m0, s89, 57344\n"
    "s_waitcnt vmcnt(3)\n"
    "buffer_load_dwordx4 v28, s[48:51], s94 offen lds\n"
    "s_add_u32 m0, m0, 0x400\n"
    "s_waitcnt vmcnt(3)\n"
    "buffer_load_dwordx4 v29, s[48:51], s94 offen lds\n"
    "s_add_u32 s94, s95, 0\n"
    "s_cmp_ge_u32 s94, 7\n"
    "s_cselect_b32 s59, 7, 0\n"
    "s_sub_u32 s94, s94, s59\n"
    "s_lshl_b32 s94, s94, 16\n"
    "s_add_u32 s94, s94, s93\n"
    "s_add_u32 m0, s89, 65536\n"
    "s_waitcnt vmcnt(3)\n"
    "buffer_load_dwordx4 v28, s[48:51], s94 offen lds\n"
    "s_add_u32 m0, m0, 0x400\n"
    "s_waitcnt vmcnt(3)\n"
    "buffer_load_dwordx4 v29, s[48:51], s94 offen lds\n"
    "s_add_u32 s94, s94, 0x4000\n"
    "s_add_u32 m0, s89, 73728\n"
    "s_waitcnt vmcnt(3)\n"
    "buffer_load_dwordx4 v28, s[48:51], s94 offen lds\n"
    "s_add_u32 m0, m0, 0x400\n"
    "s_waitcnt vmcnt(3)\n"
    "buffer_load_dwordx4 v29, s[48:51], s94 offen lds\n"
    "s_add_u32 s94, s94, 0x4000\n"
    "s_add_u32 m0, s89, 81920\n"
    "s_waitcnt vmcnt(3)\n"
    "buffer_load_dwordx4 v28, s[48:51], s94 offen lds\n"
    "s_add_u32 m0, m0, 0x400\n"
    "s_waitcnt vmcnt(3)\n"
    "buffer_load_dwordx4 v29, s[48:51], s94 offen lds\n"
    "s_add_u32 s94, s94, 0x4000\n"
    "s_add_u32 m0, s89, 90112\n"
    "s_waitcnt vmcnt(3)\n"
    "buffer_load_dwordx4 v28, s[48:51], s94 offen lds\n"
    "s_add_u32 m0, m0, 0x400\n"
    "s_waitcnt vmcnt(3)\n"
    "buffer_load_dwordx4 v29, s[48:51], s94 offen lds\n"
    "s_add_u32 s94, s95, 1\n"
    "s_cmp_ge_u32 s94, 7\n"
    "s_cselect_b32 s59, 7, 0\n"
    "s_sub_u32 s94, s94, s59\n"
    "s_lshl_b32 s94, s94, 16\n"
    "s_add_u32 s94, s94, s93\n"
    "s_add_u32 m0, s89, 98304\n"
    "s_waitcnt vmcnt(3)\n"
    "buffer_load_dwordx4 v28, s[48:51], s94 offen lds\n"
    "s_add_u32 m0, m0, 0x400\n"
    "s_waitcnt vmcnt(3)\n"
    "buffer_load_dwordx4 v29, s[48:51], s94 offen lds\n"
    "s_add_u32 s94, s94, 0x4000\n"
    "s_add_u32 m0, s89, 106496\n"
    "s_waitcnt vmcnt(3)\n"
    "buffer_load_dwordx4 v28, s[48:51], s94 offen lds\n"
    "s_add_u32 m0, m0, 0x400\n"
    "s_waitcnt vmcnt(3)\n"
    "buffer_load_dwordx4 v29, s[48:51], s94 offen lds\n"
    "s_add_u32 s94, s94, 0x4000\n"
    "s_add_u32 m0, s89, 114688\n"
    "s_waitcnt vmcnt(3)\n"
    "buffer_load_dwordx4 v28, s[48:51], s94 offen lds\n"
    "s_add_u32 m0, m0, 0x400\n"
    "s_waitcnt vmcnt(3)\n"
    "buffer_load_dwordx4 v29, s[48:51], s94 offen lds\n"
    "s_add_u32 s94, s94, 0x4000\n"
    "s_add_u32 m0, s89, 122880\n"
    "s_waitcnt vmcnt(3)\n"
    "buffer_load_dwordx4 v28, s[48:51], s94 offen lds\n"
    "s_add_u32 m0, m0, 0x400\n"
    "s_waitcnt vmcnt(3)\n"
    "buffer_load_dwordx4 v29, s[48:51], s94 offen lds\n"
    "s_add_u32 s94, s95, 2\n"
    "s_cmp_ge_u32 s94, 7\n"
    "s_cselect_b32 s59, 7, 0\n"
    "s_sub_u32 s94, s94, s59\n"
    "s_lshl_b32 s94, s94, 16\n"
    "s_add_u32 s94, s94, s93\n"
    "s_add_u32 m0, s89, 131072\n"
    "s_waitcnt vmcnt(3)\n"
    "buffer_load_dwordx4 v28, s[48:51], s94 offen lds\n"
    "s_add_u32 m0, m0, 0x400\n"
    "s_waitcnt vmcnt(3)\n"
    "buffer_load_dwordx4 v29, s[48:51], s94 offen lds\n"
    "s_add_u32 s94, s94, 0x4000\n"
    "s_add_u32 m0, s89, 139264\n"
    "s_waitcnt vmcnt(3)\n"
    "buffer_load_dwordx4 v28, s[48:51], s94 offen lds\n"
    "s_add_u32 m0, m0, 0x400\n"
    "s_waitcnt vmcnt(3)\n"
    "buffer_load_dwordx4 v29, s[48:51], s94 offen lds\n"
    "s_add_u32 s94, s94, 0x4000\n"
    "s_add_u32 m0, s89, 147456\n"
    "s_waitcnt vmcnt(3)\n"
    "buffer_load_dwordx4 v28, s[48:51], s94 offen lds\n"
    "s_add_u32 m0, m0, 0x400\n"
    "s_waitcnt vmcnt(3)\n"
    "buffer_load_dwordx4 v29, s[48:51], s94 offen lds\n"
    "s_waitcnt vmcnt(0)\n"
    "s_and_b32 s59, s53, 3\n"
    "s_lshl_b32 s59, s59, 2\n"
    "s_add_u32 s59, s59, s52\n"
    "s_lshr_b32 s62, s54, 3\n"
    "s_lshl_b32 s62, s62, 9\n"
    "s_lshl_b32 s61, s59, 15\n"
    "s_add_u32 s61, s61, s62\n"
    "v_lshrrev_b32 v16, 4, v1\n"
    "v_lshlrev_b32 v16, 10, v16\n"
    "v_bfe_u32 v17, v1, 2, 2\n"
    "v_lshl_add_u32 v16, v17, 7, v16\n"
    "buffer_load_dword v17, v16, s[36:39], s61 offen\n"
    "s_add_u32 s61, s61, 0x1000\n"
    "s_waitcnt vmcnt(2)\n"
    "buffer_load_dword v17, v16, s[36:39], s61 offen\n"
    "s_add_u32 s61, s61, 0x1000\n"
    "s_waitcnt vmcnt(2)\n"
    "buffer_load_dword v17, v16, s[36:39], s61 offen\n"
    "s_add_u32 s61, s61, 0x1000\n"
    "s_waitcnt vmcnt(2)\n"
    "buffer_load_dword v17, v16, s[36:39], s61 offen\n"
    "s_add_u32 s61, s61, 0x1000\n"
    "s_waitcnt vmcnt(2)\n"
    "buffer_load_dword v17, v16, s[36:39], s61 offen\n"
    "s_add_u32 s61, s61, 0x1000\n"
    "s_waitcnt vmcnt(2)\n"
    "buffer_load_dword v17, v16, s[36:39], s61 offen\n"
    "s_add_u32 s61, s61, 0x1000\n"
    "s_waitcnt vmcnt(2)\n"
    "buffer_load_dword v17, v16, s[36:39], s61 offen\n"
    "s_add_u32 s61, s61, 0x1000\n"
    "s_waitcnt vmcnt(2)\n"
    "buffer_load_dword v17, v16, s[36:39], s61 offen\n"
    "s_add_u32 s61, s61, 0x1000\n"
    "s_waitcnt vmcnt(2)\n"
    "s_waitcnt vmcnt(0)\n"
    "ds_read_b64_tr_b16 v[128:129], v26 offset:0\n"
    "ds_read_b64_tr_b16 v[130:131], v26 offset:512\n"
    "ds_read_b64_tr_b16 v[132:133], v26 offset:1024\n"
    "ds_read_b64_tr_b16 v[134:135], v26 offset:1536\n"
    "ds_read_b64_tr_b16 v[136:137], v26 offset:8192\n"
    "ds_read_b64_tr_b16 v[138:139], v26 offset:8704\n"
    "ds_read_b64_tr_b16 v[140:141], v26 offset:9216\n"
    "ds_read_b64_tr_b16 v[142:143], v26 offset:9728\n"
    "ds_read_b64_tr_b16 v[144:145], v26 offset:16384\n"
    "ds_read_b64_tr_b16 v[146:147], v26 offset:16896\n"
    "ds_read_b64_tr_b16 v[148:149], v26 offset:17408\n"
    "ds_read_b64_tr_b16 v[150:151], v26 offset:17920\n"
    "ds_read_b64_tr_b16 v[152:153], v26 offset:24576\n"
    "ds_read_b64_tr_b16 v[154:155], v26 offset:25088\n"
    "ds_read_b64_tr_b16 v[156:157], v26 offset:25600\n"
    "ds_read_b64_tr_b16 v[158:159], v26 offset:26112\n"
    "ds_read_b64_tr_b16 v[160:161], v26 offset:32768\n"
    "ds_read_b64_tr_b16 v[162:163], v26 offset:33280\n"
    "ds_read_b64_tr_b16 v[164:165], v26 offset:33792\n"
    "ds_read_b64_tr_b16 v[166:167], v26 offset:34304\n"
    "ds_read_b64_tr_b16 v[168:169], v26 offset:40960\n"
    "ds_read_b64_tr_b16 v[170:171], v26 offset:41472\n"
    "ds_read_b64_tr_b16 v[172:173], v26 offset:41984\n"
    "ds_read_b64_tr_b16 v[174:175], v26 offset:42496\n"
    "ds_read_b64_tr_b16 v[176:177], v26 offset:49152\n"
    "ds_read_b64_tr_b16 v[178:179], v26 offset:49664\n"
    "ds_read_b64_tr_b16 v[180:181], v26 offset:50176\n"
    "ds_read_b64_tr_b16 v[182:183], v26 offset:50688\n"
    "ds_read_b64_tr_b16 v[184:185], v26 offset:57344\n"
    "ds_read_b64_tr_b16 v[186:187], v26 offset:57856\n"
    "ds_read_b64_tr_b16 v[188:189], v26 offset:58368\n"
    "ds_read_b64_tr_b16 v[190:191], v26 offset:58880\n"
    ".LG2:\n"
    "s_setprio 3\n"
    "s_waitcnt lgkmcnt(0)\n"
    "s_mov_b64 exec, 1\n"
    "ds_add_u32 v20, v21 offset:8\n"
    "s_mov_b64 exec, -1\n"
    "s_add_u32 s94, s94, 0x4000\n"
    "s_add_u32 m0, s89, 57344\n"
    "s_nop 0\n"
    "buffer_load_dwordx4 v28, s[48:51], s94 offen lds\n"
    "s_add_u32 m0, m0, 0x400\n"
    "s_nop 0\n"
    "buffer_load_dwordx4 v29, s[48:51], s94 offen lds\n"
    "s_mov_b32 s40, s20\n"
    "s_and_b32 s41, s21, 0xffff\n"
    "s_mov_b32 s42, 2097152\n"
    "s_mov_b32 s43, 0x20000\n"
    "buffer_store_dwordx4 v[120:123], v27, s[40:43], s91 offen sc1\n"
    "buffer_store_dwordx4 v[120:123], v27, s[40:43], s74 offen sc1\n"
    "s_mov_b32 s64, 0\n"
    "s_mul_i32 s60, s52, 1792\n"
    "s_lshl_b32 s59, s53, 13\n"
    "s_add_u32 s60, s60, s59\n"
    "s_add_u32 s60, s60, 524288\n"
    "s_sleep 6\n"
    "s_mov_b32 s67, 100000\n"
    ".Lpoll13:\n"
    "v_mov_b32 v14, s56\n"
    "v_mov_b32 v15, s57\n"
    "s_mov_b32 exec_lo, 0x11111111\n"
    "s_mov_b32 exec_hi, 0x111111\n"
    "v_mov_b32 v14, 0\n"
    "v_mov_b32 v15, 0\n"
    "buffer_atomic_or_x2 v[14:15], v11, s[28:31], s60 offen sc0 sc1\n"
    "s_mov_b64 exec, -1\n"
    "s_waitcnt vmcnt(0)\n"
    "v_cmp_ne_u64 vcc, s[56:57], v[14:15]\n"
    "s_nop 1\n"
    "s_cmp_eq_u64 vcc, 0\n"
    "s_cbranch_scc1 .Lpolld13\n"
    "s_sleep 3\n"
    "s_sub_u32 s67, s67, 1\n"
    "s_cbranch_scc0 .Lpoll13\n"
    ".Lpolld13:\n"
    "s_setprio 0\n"
    "s_add_u32 s90, s95, 0\n"
    "s_cmp_ge_u32 s90, 7\n"
    "s_cselect_b32 s59, 7, 0\n"
    "s_sub_u32 s90, s90, s59\n"
    "s_lshl_b32 s90, s90, 12\n"
    "buffer_load_dwordx4 v[56:59], v24, s[28:31], s90 offen offset:0 sc1\n"
    "buffer_load_dwordx4 v[60:63], v23, s[28:31], s90 offen offset:0 sc1\n"
    "buffer_load_dwordx4 v[64:67], v24, s[28:31], s90 offen offset:1024 sc1\n"
    "buffer_load_dwordx4 v[68:71], v23, s[28:31], s90 offen offset:1024 sc1\n"
    "buffer_load_dwordx4 v[72:75], v24, s[28:31], s90 offen offset:2048 sc1\n"
    "buffer_load_dwordx4 v[76:79], v23, s[28:31], s90 offen offset:2048 sc1\n"
    "buffer_load_dwordx4 v[80:83], v24, s[28:31], s90 offen offset:3072 sc1\n"
    "buffer_load_dwordx4 v[84:87], v23, s[28:31], s90 offen offset:3072 sc1\n"
    "ds_read_b64_tr_b16 v[32:33], v25 offset:0\n"
    "ds_read_b64_tr_b16 v[34:35], v25 offset:512\n"
    "ds_read_b64_tr_b16 v[36:37], v25 offset:1024\n"
    "ds_read_b64_tr_b16 v[38:39], v25 offset:1536\n"
    "s_waitcnt vmcnt(6)\n"
    "s_waitcnt lgkmcnt(0)\n"
    "v_mfma_f32_32x32x16_bf16 v[192:207], v[32:35], v[56:59], 0\n"
    "ds_read_b64_tr_b16 v[40:41], v25 offset:8192\n"
    "ds_read_b64_tr_b16 v[42:43], v25 offset:8704\n"
    "ds_read_b64_tr_b16 v[44:45], v25 offset:9216\n"
    "ds_read_b64_tr_b16 v[46:47], v25 offset:9728\n"
    "v_mfma_f32_32x32x16_bf16 v[208:223], v[36:39], v[56:59], 0\n"
    "s_add_u32 s94, s95, 3\n"
    "s_cmp_ge_u32 s94, 7\n"
    "s_cselect_b32 s59, 7, 0\n"
    "s_sub_u32 s94, s94, s59\n"
    "s_lshl_b32 s94, s94, 16\n"
    "s_add_u32 s94, s94, s93\n"
    "s_add_u32 m0, s89, 65536\n"
    "s_nop 0\n"
    "buffer_load_dwordx4 v28, s[48:51], s94 offen lds\n"
    "s_add_u32 m0, m0, 0x400\n"
    "s_nop 0\n"
    "buffer_load_dwordx4 v29, s[48:51], s94 offen lds\n"
    "v_mfma_f32_32x32x16_bf16 v[224:239], v[32:35], v[60:63], 0\n"
    "s_add_u32 s90, s95, 1\n"
    "s_cmp_ge_u32 s90, 7\n"
    "s_cselect_b32 s59, 7, 0\n"
    "s_sub_u32 s90, s90, s59\n"
    "s_lshl_b32 s90, s90, 12\n"
    "s_waitcnt vmcnt(7)\n"
    "buffer_load_dwordx4 v[88:91], v24, s[28:31], s90 offen offset:0 sc1\n"
    "s_waitcnt vmcnt(7)\n"
    "buffer_load_dwordx4 v[92:95], v23, s[28:31], s90 offen offset:0 sc1\n"
    "v_mfma_f32_32x32x16_bf16 v[240:255], v[36:39], v[60:63], 0\n"
    "s_waitcnt lgkmcnt(0)\n"
    "v_mfma_f32_32x32x16_bf16 v[192:207], v[40:43], v[64:67], v[192:207]\n"
    "ds_read_b64_tr_b16 v[32:33], v25 offset:16384\n"
    "ds_read_b64_tr_b16 v[34:35], v25 offset:16896\n"
    "ds_read_b64_tr_b16 v[36:37], v25 offset:17408\n"
    "ds_read_b64_tr_b16 v[38:39], v25 offset:17920\n"
    "v_mfma_f32_32x32x16_bf16 v[208:223], v[44:47], v[64:67], v[208:223]\n"
    "s_add_u32 s94, s94, 0x4000\n"
    "s_add_u32 m0, s89, 73728\n"
    "s_waitcnt vmcnt(7)\n"
    "buffer_load_dwordx4 v28, s[48:51], s94 offen lds\n"
    "s_add_u32 m0, m0, 0x400\n"
    "s_waitcnt vmcnt(7)\n"
    "buffer_load_dwordx4 v29, s[48:51], s94 offen lds\n"
    "v_mfma_f32_32x32x16_bf16 v[224:239], v[40:43], v[68:71], v[224:239]\n"
    "s_waitcnt vmcnt(7)\n"
    "buffer_load_dwordx4 v[96:99], v24, s[28:31], s90 offen offset:1024 sc1\n"
    "s_waitcnt vmcnt(7)\n"
    "buffer_load_dwordx4 v[100:103], v23, s[28:31], s90 offen offset:1024 sc1\n"
    "v_mfma_f32_32x32x16_bf16 v[240:255], v[44:47], v[68:71], v[240:255]\n"
    "s_waitcnt lgkmcnt(0)\n"
    "v_mfma_f32_32x32x16_bf16 v[192:207], v[32:35], v[72:75], v[192:207]\n"
    "ds_read_b64_tr_b16 v[40:41], v25 offset:24576\n"
    "ds_read_b64_tr_b16 v[42:43], v25 offset:25088\n"
    "ds_read_b64_tr_b16 v[44:45], v25 offset:25600\n"
    "ds_read_b64_tr_b16 v[46:47], v25 offset:26112\n"
    "v_mfma_f32_32x32x16_bf16 v[208:223], v[36:39], v[72:75], v[208:223]\n"
    "s_add_u32 s94, s94, 0x4000\n"
    "s_add_u32 m0, s89, 81920\n"
    "s_waitcnt vmcnt(7)\n"
    "buffer_load_dwordx4 v28, s[48:51], s94 offen lds\n"
    "s_add_u32 m0, m0, 0x400\n"
    "s_waitcnt vmcnt(7)\n"
    "buffer_load_dwordx4 v29, s[48:51], s94 offen lds\n"
    "v_mfma_f32_32x32x16_bf16 v[224:239], v[32:35], v[76:79], v[224:239]\n"
    "s_waitcnt vmcnt(7)\n"
    "buffer_load_dwordx4 v[104:107], v24, s[28:31], s90 offen offset:2048 sc1\n"
    "s_waitcnt vmcnt(7)\n"
    "buffer_load_dwordx4 v[108:111], v23, s[28:31], s90 offen offset:2048 sc1\n"
    "v_mfma_f32_32x32x16_bf16 v[240:255], v[36:39], v[76:79], v[240:255]\n"
    "s_waitcnt lgkmcnt(0)\n"
    "v_mfma_f32_32x32x16_bf16 v[192:207], v[40:43], v[80:83], v[192:207]\n"
    "ds_read_b64_tr_b16 v[32:33], v25 offset:32768\n"
    "ds_read_b64_tr_b16 v[34:35], v25 offset:33280\n"
    "ds_read_b64_tr_b16 v[36:37], v25 offset:33792\n"
    "ds_read_b64_tr_b16 v[38:39], v25 offset:34304\n"
    "v_mfma_f32_32x32x16_bf16 v[208:223], v[44:47], v[80:83], v[208:223]\n"
    "s_add_u32 s94, s94, 0x4000\n"
    "s_add_u32 m0, s89, 90112\n"
    "s_waitcnt vmcnt(7)\n"
    "buffer_load_dwordx4 v28, s[48:51], s94 offen lds\n"
    "s_add_u32 m0, m0, 0x400\n"
    "s_waitcnt vmcnt(7)\n"
    "buffer_load_dwordx4 v29, s[48:51], s94 offen lds\n"
    "v_mfma_f32_32x32x16_bf16 v[224:239], v[40:43], v[84:87], v[224:239]\n"
    "s_waitcnt vmcnt(7)\n"
    "buffer_load_dwordx4 v[112:115], v24, s[28:31], s90 offen offset:3072 sc1\n"
    "s_waitcnt vmcnt(7)\n"
    "buffer_load_dwordx4 v[116:119], v23, s[28:31], s90 offen offset:3072 sc1\n"
    "v_mfma_f32_32x32x16_bf16 v[240:255], v[44:47], v[84:87], v[240:255]\n"
    "s_waitcnt lgkmcnt(0)\n"
    "v_mfma_f32_32x32x16_bf16 v[192:207], v[32:35], v[88:91], v[192:207]\n"
    "ds_read_b64_tr_b16 v[40:41], v25 offset:40960\n"
    "ds_read_b64_tr_b16 v[42:43], v25 offset:41472\n"
    "ds_read_b64_tr_b16 v[44:45], v25 offset:41984\n"
    "ds_read_b64_tr_b16 v[46:47], v25 offset:42496\n"
    "v_mfma_f32_32x32x16_bf16 v[208:223], v[36:39], v[88:91], v[208:223]\n"
    "s_add_u32 s94, s95, 4\n"
    "s_cmp_ge_u32 s94, 7\n"
    "s_cselect_b32 s59, 7, 0\n"
    "s_sub_u32 s94, s94, s59\n"
    "s_lshl_b32 s94, s94, 16\n"
    "s_add_u32 s94, s94, s93\n"
    "s_add_u32 m0, s89, 98304\n"
    "s_waitcnt vmcnt(7)\n"
    "buffer_load_dwordx4 v28, s[48:51], s94 offen lds\n"
    "s_add_u32 m0, m0, 0x400\n"
    "s_waitcnt vmcnt(7)\n"
    "buffer_load_dwordx4 v29, s[48:51], s94 offen lds\n"
    "v_mfma_f32_32x32x16_bf16 v[224:239], v[32:35], v[92:95], v[224:239]\n"
    "s_add_u32 s90, s95, 2\n"
    "s_cmp_ge_u32 s90, 7\n"
    "s_cselect_b32 s59, 7, 0\n"
    "s_sub_u32 s90, s90, s59\n"
    "s_lshl_b32 s90, s90, 12\n"
    "s_waitcnt vmcnt(7)\n"
    "buffer_load_dwordx4 v[56:59], v24, s[28:31], s90 offen offset:0 sc1\n"
    "s_waitcnt vmcnt(7)\n"
    "buffer_load_dwordx4 v[60:63], v23, s[28:31], s90 offen offset:0 sc1\n"
    "v_mfma_f32_32x32x16_bf16 v[240:255], v[36:39], v[92:95], v[240:255]\n"
    "s_waitcnt lgkmcnt(0)\n"
    "v_mfma_f32_32x32x16_bf16 v[192:207], v[40:43], v[96:99], v[192:207]\n"
    "ds_read_b64_tr_b16 v[32:33], v25 offset:49152\n"
    "ds_read_b64_tr_b16 v[34:35], v25 offset:49664\n"
    "ds_read_b64_tr_b16 v[36:37], v25 offset:50176\n"
    "ds_read_b64_tr_b16 v[38:39], v25 offset:50688\n"
    "v_mfma_f32_32x32x16_bf16 v[208:223], v[44:47], v[96:99], v[208:223]\n"
    "s_add_u32 s94, s94, 0x4000\n"
    "s_add_u32 m0, s89, 106496\n"
    "s_waitcnt vmcnt(7)\n"
    "buffer_load_dwordx4 v28, s[48:51], s94 offen lds\n"
    "s_add_u32 m0, m0, 0x400\n"
    "s_waitcnt vmcnt(7)\n"
    "buffer_load_dwordx4 v29, s[48:51], s94 offen lds\n"
    "v_mfma_f32_32x32x16_bf16 v[224:239], v[40:43], v[100:103], v[224:239]\n"
    "s_waitcnt vmcnt(7)\n"
    "buffer_load_dwordx4 v[64:67], v24, s[28:31], s90 offen offset:1024 sc1\n"
    "s_waitcnt vmcnt(7)\n"
    "buffer_load_dwordx4 v[68:71], v23, s[28:31], s90 offen offset:1024 sc1\n"
    "v_mfma_f32_32x32x16_bf16 v[240:255], v[44:47], v[100:103], v[240:255]\n"
    "s_waitcnt lgkmcnt(0)\n"
    "v_mfma_f32_32x32x16_bf16 v[192:207], v[32:35], v[104:107], v[192:207]\n"
    "ds_read_b64_tr_b16 v[40:41], v25 offset:57344\n"
    "ds_read_b64_tr_b16 v[42:43], v25 offset:57856\n"
    "ds_read_b64_tr_b16 v[44:45], v25 offset:58368\n"
    "ds_read_b64_tr_b16 v[46:47], v25 offset:58880\n"
    "v_mfma_f32_32x32x16_bf16 v[208:223], v[36:39], v[104:107], v[208:223]\n"
    "s_add_u32 s94, s94, 0x4000\n"
    "s_add_u32 m0, s89, 114688\n"
    "s_waitcnt vmcnt(7)\n"
    "buffer_load_dwordx4 v28, s[48:51], s94 offen lds\n"
    "s_add_u32 m0, m0, 0x400\n"
    "s_waitcnt vmcnt(7)\n"
    "buffer_load_dwordx4 v29, s[48:51], s94 offen lds\n"
    "v_mfma_f32_32x32x16_bf16 v[224:239], v[32:35], v[108:111], v[224:239]\n"
    "s_waitcnt vmcnt(7)\n"
    "buffer_load_dwordx4 v[72:75], v24, s[28:31], s90 offen offset:2048 sc1\n"
    "s_waitcnt vmcnt(7)\n"
    "buffer_load_dwordx4 v[76:79], v23, s[28:31], s90 offen offset:2048 sc1\n"
    "v_mfma_f32_32x32x16_bf16 v[240:255], v[36:39], v[108:111], v[240:255]\n"
    "s_waitcnt lgkmcnt(0)\n"
    "v_mfma_f32_32x32x16_bf16 v[192:207], v[40:43], v[112:115], v[192:207]\n"
    "ds_read_b64_tr_b16 v[32:33], v22 offset:0\n"
    "ds_read_b64_tr_b16 v[34:35], v22 offset:512\n"
    "ds_read_b64_tr_b16 v[36:37], v22 offset:1024\n"
    "ds_read_b64_tr_b16 v[38:39], v22 offset:1536\n"
    "v_mfma_f32_32x32x16_bf16 v[208:223], v[44:47], v[112:115], v[208:223]\n"
    "s_add_u32 s94, s94, 0x4000\n"
    "s_add_u32 m0, s89, 122880\n"
    "s_waitcnt vmcnt(7)\n"
    "buffer_load_dwordx4 v28, s[48:51], s94 offen lds\n"
    "s_add_u32 m0, m0, 0x400\n"
    "s_waitcnt vmcnt(7)\n"
    "buffer_load_dwordx4 v29, s[48:51], s94 offen lds\n"
    "v_mfma_f32_32x32x16_bf16 v[224:239], v[40:43], v[116:119], v[224:239]\n"
    "s_waitcnt vmcnt(7)\n"
    "buffer_load_dwordx4 v[80:83], v24, s[28:31], s90 offen offset:3072 sc1\n"
    "s_waitcnt vmcnt(7)\n"
    "buffer_load_dwordx4 v[84:87], v23, s[28:31], s90 offen offset:3072 sc1\n"
    "v_mfma_f32_32x32x16_bf16 v[240:255], v[44:47], v[116:119], v[240:255]\n"
    "s_waitcnt lgkmcnt(0)\n"
    "v_mfma_f32_32x32x16_bf16 v[192:207], v[32:35], v[56:59], v[192:207]\n"
    "ds_read_b64_tr_b16 v[40:41], v22 offset:8192\n"
    "ds_read_b64_tr_b16 v[42:43], v22 offset:8704\n"
    "ds_read_b64_tr_b16 v[44:45], v22 offset:9216\n"
    "ds_read_b64_tr_b16 v[46:47], v22 offset:9728\n"
    "v_mfma_f32_32x32x16_bf16 v[208:223], v[36:39], v[56:59], v[208:223]\n"
    "s_add_u32 s94, s54, 0\n"
    "s_and_b32 s94, s94, 6\n"
    "s_lshl_b32 s59, s52, 3\n"
    "s_add_u32 s94, s94, s59\n"
    "s_lshl_b32 s94, s94, 14\n"
    "s_add_u32 m0, s89, 131072\n"
    "s_waitcnt vmcnt(7)\n"
    "buffer_load_dwordx4 v28, s[36:39], s94 offen lds\n"
    "s_add_u32 m0, m0, 0x400\n"
    "s_waitcnt vmcnt(7)\n"
    "buffer_load_dwordx4 v29, s[36:39], s94 offen lds\n"
    "v_mfma_f32_32x32x16_bf16 v[224:239], v[32:35], v[60:63], v[224:239]\n"
    "s_add_u32 s90, s95, 3\n"
    "s_cmp_ge_u32 s90, 7\n"
    "s_cselect_b32 s59, 7, 0\n"
    "s_sub_u32 s90, s90, s59\n"
    "s_lshl_b32 s90, s90, 12\n"
    "s_waitcnt vmcnt(7)\n"
    "buffer_load_dwordx4 v[88:91], v24, s[28:31], s90 offen offset:0 sc1\n"
    "s_waitcnt vmcnt(7)\n"
    "buffer_load_dwordx4 v[92:95], v23, s[28:31], s90 offen offset:0 sc1\n"
    "v_mfma_f32_32x32x16_bf16 v[240:255], v[36:39], v[60:63], v[240:255]\n"
    "s_waitcnt lgkmcnt(0)\n"
    "v_mfma_f32_32x32x16_bf16 v[192:207], v[40:43], v[64:67], v[192:207]\n"
    "ds_read_b64_tr_b16 v[32:33], v22 offset:16384\n"
    "ds_read_b64_tr_b16 v[34:35], v22 offset:16896\n"
    "ds_read_b64_tr_b16 v[36:37], v22 offset:17408\n"
    "ds_read_b64_tr_b16 v[38:39], v22 offset:17920\n"
    "v_mfma_f32_32x32x16_bf16 v[208:223], v[44:47], v[64:67], v[208:223]\n"
    "s_add_u32 s94, s94, 0x4000\n"
    "s_add_u32 m0, s89, 139264\n"
    "s_waitcnt vmcnt(7)\n"
    "buffer_load_dwordx4 v28, s[36:39], s94 offen lds\n"
    "s_add_u32 m0, m0, 0x400\n"
    "s_waitcnt vmcnt(7)\n"
    "buffer_load_dwordx4 v29, s[36:39], s94 offen lds\n"
    "v_mfma_f32_32x32x16_bf16 v[224:239], v[40:43], v[68:71], v[224:239]\n"
    "s_waitcnt vmcnt(7)\n"
    "buffer_load_dwordx4 v[96:99], v24, s[28:31], s90 offen offset:1024 sc1\n"
    "s_waitcnt vmcnt(7)\n"
    "buffer_load_dwordx4 v[100:103], v23, s[28:31], s90 offen offset:1024 sc1\n"
    "v_mfma_f32_32x32x16_bf16 v[240:255], v[44:47], v[68:71], v[240:255]\n"
    "s_waitcnt lgkmcnt(0)\n"
    "v_mfma_f32_32x32x16_bf16 v[192:207], v[32:35], v[72:75], v[192:207]\n"
    "ds_read_b64_tr_b16 v[40:41], v26 offset:57344\n"
    "ds_read_b64_tr_b16 v[42:43], v26 offset:57856\n"
    "ds_read_b64_tr_b16 v[44:45], v26 offset:58368\n"
    "ds_read_b64_tr_b16 v[46:47], v26 offset:58880\n"
    "v_mfma_f32_32x32x16_bf16 v[208:223], v[36:39], v[72:75], v[208:223]\n"
    "s_add_u32 s94, s54, 2\n"
    "s_and_b32 s94, s94, 6\n"
    "s_lshl_b32 s59, s52, 3\n"
    "s_add_u32 s94, s94, s59\n"
    "s_lshl_b32 s94, s94, 14\n"
    "s_add_u32 m0, s89, 147456\n"
    "s_waitcnt vmcnt(7)\n"
    "buffer_load_dwordx4 v28, s[36:39], s94 offen lds\n"
    "s_add_u32 m0, m0, 0x400\n"
    "s_waitcnt vmcnt(7)\n"
    "buffer_load_dwordx4 v29, s[36:39], s94 offen lds\n"
    "v_mfma_f32_32x32x16_bf16 v[224:239], v[32:35], v[76:79], v[224:239]\n"
    "s_waitcnt vmcnt(7)\n"
    "buffer_load_dwordx4 v[104:107], v24, s[28:31], s90 offen offset:2048 sc1\n"
    "s_waitcnt vmcnt(7)\n"
    "buffer_load_dwordx4 v[108:111], v23, s[28:31], s90 offen offset:2048 sc1\n"
    "v_mfma_f32_32x32x16_bf16 v[240:255], v[36:39], v[76:79], v[240:255]\n"
    "s_waitcnt lgkmcnt(0)\n"
    "v_mfma_f32_32x32x16_bf16 v[192:207], v[40:43], v[80:83], v[192:207]\n"
    "ds_read_b64_tr_b16 v[32:33], v25 offset:0\n"
    "ds_read_b64_tr_b16 v[34:35], v25 offset:512\n"
    "ds_read_b64_tr_b16 v[36:37], v25 offset:1024\n"
    "ds_read_b64_tr_b16 v[38:39], v25 offset:1536\n"
    "v_mfma_f32_32x32x16_bf16 v[208:223], v[44:47], v[80:83], v[208:223]\n"
    "s_add_u32 s94, s94, 0x4000\n"
    "s_add_u32 m0, s89, 57344\n"
    "s_waitcnt vmcnt(7)\n"
    "buffer_load_dwordx4 v28, s[36:39], s94 offen lds\n"
    "s_add_u32 m0, m0, 0x400\n"
    "s_waitcnt vmcnt(7)\n"
    "buffer_load_dwordx4 v29, s[36:39], s94 offen lds\n"
    "v_mfma_f32_32x32x16_bf16 v[224:239], v[40:43], v[84:87], v[224:239]\n"
    "s_waitcnt vmcnt(7)\n"
    "buffer_load_dwordx4 v[112:115], v24, s[28:31], s90 offen offset:3072 sc1\n"
    "s_waitcnt vmcnt(7)\n"
    "buffer_load_dwordx4 v[116:119], v23, s[28:31], s90 offen offset:3072 sc1\n"
    "v_mfma_f32_32x32x16_bf16 v[240:255], v[44:47], v[84:87], v[240:255]\n"
    "s_waitcnt lgkmcnt(0)\n"
    "v_mfma_f32_32x32x16_bf16 v[192:207], v[32:35], v[88:91], v[192:207]\n"
    "ds_read_b64_tr_b16 v[40:41], v25 offset:8192\n"
    "ds_read_b64_tr_b16 v[42:43], v25 offset:8704\n"
    "ds_read_b64_tr_b16 v[44:45], v25 offset:9216\n"
    "ds_read_b64_tr_b16 v[46:47], v25 offset:9728\n"
    "v_mfma_f32_32x32x16_bf16 v[208:223], v[36:39], v[88:91], v[208:223]\n"
    "s_add_u32 s94, s54, 4\n"
    "s_and_b32 s94, s94, 6\n"
    "s_lshl_b32 s59, s52, 3\n"
    "s_add_u32 s94, s94, s59\n"
    "s_lshl_b32 s94, s94, 14\n"
    "s_add_u32 m0, s89, 65536\n"
    "s_waitcnt vmcnt(7)\n"
    "buffer_load_dwordx4 v28, s[36:39], s94 offen lds\n"
    "s_add_u32 m0, m0, 0x400\n"
    "s_waitcnt vmcnt(7)\n"
    "buffer_load_dwordx4 v29, s[36:39], s94 offen lds\n"
    "v_mfma_f32_32x32x16_bf16 v[224:239], v[32:35], v[92:95], v[224:239]\n"
    "s_add_u32 s90, s95, 4\n"
    "s_cmp_ge_u32 s90, 7\n"
    "s_cselect_b32 s59, 7, 0\n"
    "s_sub_u32 s90, s90, s59\n"
    "s_lshl_b32 s90, s90, 12\n"
    "s_waitcnt vmcnt(7)\n"
    "buffer_load_dwordx4 v[56:59], v24, s[28:31], s90 offen offset:0 sc1\n"
    "s_waitcnt vmcnt(7)\n"
    "buffer_load_dwordx4 v[60:63], v23, s[28:31], s90 offen offset:0 sc1\n"
    "v_mfma_f32_32x32x16_bf16 v[240:255], v[36:39], v[92:95], v[240:255]\n"
    "s_waitcnt lgkmcnt(0)\n"
    "v_mfma_f32_32x32x16_bf16 v[192:207], v[40:43], v[96:99], v[192:207]\n"
    "ds_read_b64_tr_b16 v[32:33], v25 offset:16384\n"
    "ds_read_b64_tr_b16 v[34:35], v25 offset:16896\n"
    "ds_read_b64_tr_b16 v[36:37], v25 offset:17408\n"
    "ds_read_b64_tr_b16 v[38:39], v25 offset:17920\n"
    "v_mfma_f32_32x32x16_bf16 v[208:223], v[44:47], v[96:99], v[208:223]\n"
    "s_add_u32 s94, s94, 0x4000\n"
    "s_add_u32 m0, s89, 73728\n"
    "s_waitcnt vmcnt(7)\n"
    "buffer_load_dwordx4 v28, s[36:39], s94 offen lds\n"
    "s_add_u32 m0, m0, 0x400\n"
    "s_waitcnt vmcnt(7)\n"
    "buffer_load_dwordx4 v29, s[36:39], s94 offen lds\n"
    "v_mfma_f32_32x32x16_bf16 v[224:239], v[40:43], v[100:103], v[224:239]\n"
    "s_waitcnt vmcnt(7)\n"
    "buffer_load_dwordx4 v[64:67], v24, s[28:31], s90 offen offset:1024 sc1\n"
    "s_waitcnt vmcnt(7)\n"
    "buffer_load_dwordx4 v[68:71], v23, s[28:31], s90 offen offset:1024 sc1\n"
    "v_mfma_f32_32x32x16_bf16 v[240:255], v[44:47], v[100:103], v[240:255]\n"
    "s_waitcnt lgkmcnt(0)\n"
    "v_mfma_f32_32x32x16_bf16 v[192:207], v[32:35], v[104:107], v[192:207]\n"
    "ds_read_b64_tr_b16 v[40:41], v25 offset:24576\n"
    "ds_read_b64_tr_b16 v[42:43], v25 offset:25088\n"
    "ds_read_b64_tr_b16 v[44:45], v25 offset:25600\n"
    "ds_read_b64_tr_b16 v[46:47], v25 offset:26112\n"
    "v_mfma_f32_32x32x16_bf16 v[208:223], v[36:39], v[104:107], v[208:223]\n"
    "s_add_u32 s94, s54, 6\n"
    "s_and_b32 s94, s94, 6\n"
    "s_lshl_b32 s59, s52, 3\n"
    "s_add_u32 s94, s94, s59\n"
    "s_lshl_b32 s94, s94, 14\n"
    "s_add_u32 m0, s89, 81920\n"
    "s_waitcnt vmcnt(7)\n"
    "buffer_load_dwordx4 v28, s[36:39], s94 offen lds\n"
    "s_add_u32 m0, m0, 0x400\n"
    "s_waitcnt vmcnt(7)\n"
    "buffer_load_dwordx4 v29, s[36:39], s94 offen lds\n"
    "v_mfma_f32_32x32x16_bf16 v[224:239], v[32:35], v[108:111], v[224:239]\n"
    "s_waitcnt vmcnt(7)\n"
    "buffer_load_dwordx4 v[72:75], v24, s[28:31], s90 offen offset:2048 sc1\n"
    "s_waitcnt vmcnt(7)\n"
    "buffer_load_dwordx4 v[76:79], v23, s[28:31], s90 offen offset:2048 sc1\n"
    "v_mfma_f32_32x32x16_bf16 v[240:255], v[36:39], v[108:111], v[240:255]\n"
    "s_waitcnt lgkmcnt(0)\n"
    "v_mfma_f32_32x32x16_bf16 v[192:207], v[40:43], v[112:115], v[192:207]\n"
    "ds_read_b64_tr_b16 v[32:33], v25 offset:32768\n"
    "ds_read_b64_tr_b16 v[34:35], v25 offset:33280\n"
    "ds_read_b64_tr_b16 v[36:37], v25 offset:33792\n"
    "ds_read_b64_tr_b16 v[38:39], v25 offset:34304\n"
    "v_mfma_f32_32x32x16_bf16 v[208:223], v[44:47], v[112:115], v[208:223]\n"
    "s_add_u32 s94, s94, 0x4000\n"
    "s_add_u32 m0, s89, 90112\n"
    "s_waitcnt vmcnt(7)\n"
    "buffer_load_dwordx4 v28, s[36:39], s94 offen lds\n"
    "s_add_u32 m0, m0, 0x400\n"
    "s_waitcnt vmcnt(7)\n"
    "buffer_load_dwordx4 v29, s[36:39], s94 offen lds\n"
    "v_mfma_f32_32x32x16_bf16 v[224:239], v[40:43], v[116:119], v[224:239]\n"
    "s_waitcnt vmcnt(7)\n"
    "buffer_load_dwordx4 v[80:83], v24, s[28:31], s90 offen offset:3072 sc1\n"
    "s_waitcnt vmcnt(7)\n"
    "buffer_load_dwordx4 v[84:87], v23, s[28:31], s90 offen offset:3072 sc1\n"
    "v_mfma_f32_32x32x16_bf16 v[240:255], v[44:47], v[116:119], v[240:255]\n"
    "s_waitcnt lgkmcnt(0)\n"
    "s_add_u32 s90, s95, 5\n"
    "s_cmp_ge_u32 s90, 7\n"
    "s_cselect_b32 s59, 7, 0\n"
    "s_sub_u32 s90, s90, s59\n"
    "s_lshl_b32 s90, s90, 12\n"
    "s_waitcnt vmcnt(7)\n"
    "buffer_load_dwordx4 v[88:91], v24, s[28:31], s90 offen offset:0 sc1\n"
    "s_waitcnt vmcnt(7)\n"
    "buffer_load_dwordx4 v[92:95], v23, s[28:31], s90 offen offset:0 sc1\n"
    "s_waitcnt vmcnt(7)\n"
    "buffer_load_dwordx4 v[96:99], v24, s[28:31], s90 offen offset:1024 sc1\n"
    "s_waitcnt vmcnt(7)\n"
    "buffer_load_dwordx4 v[100:103], v23, s[28:31], s90 offen offset:1024 sc1\n"
    "s_waitcnt vmcnt(7)\n"
    "buffer_load_dwordx4 v[104:107], v24, s[28:31], s90 offen offset:2048 sc1\n"
    "s_waitcnt vmcnt(7)\n"
    "buffer_load_dwordx4 v[108:111], v23, s[28:31], s90 offen offset:2048 sc1\n"
    "s_waitcnt vmcnt(7)\n"
    "buffer_load_dwordx4 v[112:115], v24, s[28:31], s90 offen offset:3072 sc1\n"
    "s_waitcnt vmcnt(7)\n"
    "buffer_load_dwordx4 v[116:119], v23, s[28:31], s90 offen offset:3072 sc1\n"
    "s_setprio 3\n"
    "s_mov_b32 s67, 400000\n"
    ".Lpoll14:\n"
    "ds_read_b32 v13, v20 offset:12\n"
    "s_waitcnt lgkmcnt(0)\n"
    "v_readfirstlane_b32 s59, v13\n"
    "s_cmp_eq_u32 s59, 4\n"
    "s_cbranch_scc1 .Lpolld14\n"
    "s_sleep 2\n"
    "s_sub_u32 s67, s67, 1\n"
    "s_cbranch_scc0 .Lpoll14\n"
    ".Lpolld14:\n"
    "s_setprio 1\n"
    "v_mfma_f32_32x32x16_bf16 v[192:207], v[32:35], v[56:59], v[192:207]\n"
    "ds_read_b64_tr_b16 v[40:41], v25 offset:40960\n"
    "ds_read_b64_tr_b16 v[42:43], v25 offset:41472\n"
    "ds_read_b64_tr_b16 v[44:45], v25 offset:41984\n"
    "ds_read_b64_tr_b16 v[46:47], v25 offset:42496\n"
    "v_mfma_f32_32x32x16_bf16 v[208:223], v[36:39], v[56:59], v[208:223]\n"
    "v_mfma_f32_32x32x16_bf16 v[224:239], v[32:35], v[60:63], v[224:239]\n"
    "v_mfma_f32_32x32x16_bf16 v[240:255], v[36:39], v[60:63], v[240:255]\n"
    "s_waitcnt lgkmcnt(0)\n"
    "v_mfma_f32_32x32x16_bf16 v[192:207], v[40:43], v[64:67], v[192:207]\n"
    "ds_read_b64_tr_b16 v[32:33], v25 offset:49152\n"
    "ds_read_b64_tr_b16 v[34:35], v25 offset:49664\n"
    "ds_read_b64_tr_b16 v[36:37], v25 offset:50176\n"
    "ds_read_b64_tr_b16 v[38:39], v25 offset:50688\n"
    "v_mfma_f32_32x32x16_bf16 v[208:223], v[44:47], v[64:67], v[208:223]\n"
    "s_add_u32 s90, s95, 6\n"
    "s_cmp_ge_u32 s90, 7\n"
    "s_cselect_b32 s59, 7, 0\n"
    "s_sub_u32 s90, s90, s59\n"
    "s_lshl_b32 s90, s90, 12\n"
    "buffer_load_dwordx4 v[56:59], v24, s[28:31], s90 offen offset:0 sc1\n"
    "buffer_load_dwordx4 v[60:63], v23, s[28:31], s90 offen offset:0 sc1\n"
    "v_mfma_f32_32x32x16_bf16 v[224:239], v[40:43], v[68:71], v[224:239]\n"
    "s_lshl_b32 s61, s52, 3\n"
    "s_lshl_b32 s60, s92, 5\n"
    "s_add_u32 s61, s61, s60\n"
    "s_lshl_b32 s61, s61, 10\n"
    "v_mfma_f32_32x32x16_bf16 v[240:255], v[44:47], v[68:71], v[240:255]\n"
    "s_xor_b32 s62, s61, 0x2000\n"
    "s_add_u32 s61, s61, 1048576\n"
    "s_add_u32 s62, s62, 1048576\n"
    "v_lshrrev_b32 v19, 3, v1\n"
    "s_waitcnt lgkmcnt(0)\n"
    "v_mfma_f32_32x32x16_bf16 v[192:207], v[32:35], v[72:75], v[192:207]\n"
    "ds_read_b64_tr_b16 v[40:41], v25 offset:57344\n"
    "ds_read_b64_tr_b16 v[42:43], v25 offset:57856\n"
    "ds_read_b64_tr_b16 v[44:45], v25 offset:58368\n"
    "ds_read_b64_tr_b16 v[46:47], v25 offset:58880\n"
    "v_mfma_f32_32x32x16_bf16 v[208:223], v[36:39], v[72:75], v[208:223]\n"
    "s_waitcnt vmcnt(9)\n"
    "buffer_load_dwordx4 v[64:67], v24, s[28:31], s90 offen offset:1024 sc1\n"
    "s_waitcnt vmcnt(9)\n"
    "buffer_load_dwordx4 v[68:71], v23, s[28:31], s90 offen offset:1024 sc1\n"
    "v_mfma_f32_32x32x16_bf16 v[224:239], v[32:35], v[76:79], v[224:239]\n"
    "v_lshlrev_b32 v19, 7, v19\n"
    "v_mul_f32 v31, v31, v31\n"
    "s_lshl_b32 s63, s92, 1\n"
    "s_lshr_b32 s60, s52, 1\n"
    "v_mfma_f32_32x32x16_bf16 v[240:255], v[36:39], v[76:79], v[240:255]\n"
    "s_add_u32 s63, s63, s60\n"
    "s_lshl_b32 s63, s63, 10\n"
    "s_add_u32 s63, s63, 655360\n"
    "s_add_u32 s60, s54, 0\n"
    "s_waitcnt lgkmcnt(0)\n"
    "v_mfma_f32_32x32x16_bf16 v[192:207], v[40:43], v[80:83], v[192:207]\n"
    "v_mfma_f32_32x32x16_bf16 v[208:223], v[44:47], v[80:83], v[208:223]\n"
    "s_waitcnt vmcnt(9)\n"
    "buffer_load_dwordx4 v[72:75], v24, s[28:31], s90 offen offset:2048 sc1\n"
    "s_waitcnt vmcnt(9)\n"
    "buffer_load_dwordx4 v[76:79], v23, s[28:31], s90 offen offset:2048 sc1\n"
    "v_mfma_f32_32x32x16_bf16 v[224:239], v[40:43], v[84:87], v[224:239]\n"
    "s_and_b32 s60, s60, 6\n"
    "s_lshl_b32 s60, s60, 10\n"
    "v_add_u32 v48, s60, v10\n"
    "s_add_u32 s60, s54, 2\n"
    "v_mfma_f32_32x32x16_bf16 v[240:255], v[44:47], v[84:87], v[240:255]\n"
    "s_and_b32 s60, s60, 6\n"
    "s_lshl_b32 s60, s60, 10\n"
    "v_add_u32 v49, s60, v10\n"
    "s_add_u32 s60, s54, 4\n"
    "s_waitcnt lgkmcnt(0)\n"
    "v_mfma_f32_32x32x16_bf16 v[192:207], v[128:131], v[88:91], v[192:207]\n"
    "v_mfma_f32_32x32x16_bf16 v[208:223], v[132:135], v[88:91], v[208:223]\n"
    "s_waitcnt vmcnt(9)\n"
    "buffer_load_dwordx4 v[80:83], v24, s[28:31], s90 offen offset:3072 sc1\n"
    "s_waitcnt vmcnt(9)\n"
    "buffer_load_dwordx4 v[84:87], v23, s[28:31], s90 offen offset:3072 sc1\n"
    "v_mfma_f32_32x32x16_bf16 v[224:239], v[128:131], v[92:95], v[224:239]\n"
    "s_and_b32 s60, s60, 6\n"
    "s_lshl_b32 s60, s60, 10\n"
    "v_add_u32 v50, s60, v10\n"
    "s_add_u32 s60, s54, 6\n"
    "v_mfma_f32_32x32x16_bf16 v[240:255], v[132:135], v[92:95], v[240:255]\n"
    "s_and_b32 s60, s60, 6\n"
    "s_lshl_b32 s60, s60, 10\n"
    "v_add_u32 v51, s60, v10\n"
    "s_mul_i32 s60, s52, 12288\n"
    "v_mfma_f32_32x32x16_bf16 v[192:207], v[136:139], v[96:99], v[192:207]\n"
    "ds_read_b64_tr_b16 v[88:89], v22 offset:0\n"
    "ds_read_b64_tr_b16 v[90:91], v22 offset:512\n"
    "ds_read_b64_tr_b16 v[92:93], v22 offset:1024\n"
    "ds_read_b64_tr_b16 v[94:95], v22 offset:1536\n"
    "v_mfma_f32_32x32x16_bf16 v[208:223], v[140:143], v[96:99], v[208:223]\n"
    "v_add_u32 v12, s60, v10\n"
    "s_xor_b32 s60, s52, 1\n"
    "s_mul_i32 s60, s60, 12288\n"
    "v_add_u32 v16, s60, v10\n"
    "v_mfma_f32_32x32x16_bf16 v[224:239], v[136:139], v[100:103], v[224:239]\n"
    "s_xor_b32 s60, s52, 2\n"
    "s_mul_i32 s60, s60, 12288\n"
    "v_add_u32 v17, s60, v10\n"
    "s_xor_b32 s60, s52, 3\n"
    "v_mfma_f32_32x32x16_bf16 v[240:255], v[140:143], v[100:103], v[240:255]\n"
    "s_mul_i32 s60, s60, 12288\n"
    "v_add_u32 v18, s60, v10\n"
    "v_xor_b32 v2, 0x800, v26\n"
    "v_xor_b32 v3, 0x800, v25\n"
    "v_mov_b32 v14, s56\n"
    "v_mov_b32 v15, s57\n"
    "s_mov_b32 exec_lo, 0x1010101\n"
    "s_mov_b32 exec_hi, 0x1010101\n"
    "v_mov_b32 v14, 0\n"
    "v_mov_b32 v15, 0\n"
    "buffer_atomic_or_x2 v[14:15], v19, s[28:31], s63 offen sc0 sc1\n"
    "s_mov_b64 exec, -1\n"
    "v_mfma_f32_32x32x16_bf16 v[192:207], v[144:147], v[104:107], v[192:207]\n"
    "ds_read_b64_tr_b16 v[96:97], v22 offset:8192\n"
    "ds_read_b64_tr_b16 v[98:99], v22 offset:8704\n"
    "ds_read_b64_tr_b16 v[100:101], v22 offset:9216\n"
    "ds_read_b64_tr_b16 v[102:103], v22 offset:9728\n"
    "v_mfma_f32_32x32x16_bf16 v[208:223], v[148:151], v[104:107], v[208:223]\n"
    "v_xor_b32 v4, 0x800, v22\n"
    "s_and_b32 s75, s52, 1\n"
    "s_lshl_b32 s75, s75, 12\n"
    "s_add_u32 s75, s75, 49152\n"
    "v_mfma_f32_32x32x16_bf16 v[224:239], v[144:147], v[108:111], v[224:239]\n"
    "s_lshr_b32 s60, s52, 1\n"
    "s_lshl_b32 s60, s60, 2\n"
    "v_and_b32 v13, 7, v6\n"
    "v_xor_b32 v13, s60, v13\n"
    "v_mfma_f32_32x32x16_bf16 v[240:255], v[148:151], v[108:111], v[240:255]\n"
    "v_lshlrev_b32 v13, 4, v13\n"
    "v_lshl_add_u32 v13, v6, 7, v13\n"
    "v_lshl_add_u32 v13, v7, 3, v13\n"
    "v_add_u32 v52, s75, v13\n"
    "s_waitcnt vmcnt(9)\n"
    "v_mfma_f32_32x32x16_bf16 v[192:207], v[152:155], v[112:115], v[192:207]\n"
    "ds_read_b64_tr_b16 v[104:105], v22 offset:16384\n"
    "ds_read_b64_tr_b16 v[106:107], v22 offset:16896\n"
    "ds_read_b64_tr_b16 v[108:109], v22 offset:17408\n"
    "ds_read_b64_tr_b16 v[110:111], v22 offset:17920\n"
    "v_mfma_f32_32x32x16_bf16 v[208:223], v[156:159], v[112:115], v[208:223]\n"
    "v_xor_b32 v53, 16, v52\n"
    "v_xor_b32 v54, 32, v52\n"
    "v_xor_b32 v55, 48, v52\n"
    "v_lshrrev_b32 v13, 3, v1\n"
    "v_mfma_f32_32x32x16_bf16 v[224:239], v[152:155], v[116:119], v[224:239]\n"
    "v_lshlrev_b32 v13, 4, v13\n"
    "v_xor_b32 v13, v13, v10\n"
    "s_lshl_b32 s75, s52, 11\n"
    "s_add_u32 s75, s75, 49152\n"
    "v_mfma_f32_32x32x16_bf16 v[240:255], v[156:159], v[116:119], v[240:255]\n"
    "v_add_u32 v30, s75, v13\n"
    "v_lshlrev_b32 v32, 16, v120\n"
    "v_and_b32 v33, 0xffff0000, v120\n"
    "v_lshlrev_b32 v34, 16, v121\n"
    "s_waitcnt vmcnt(7)\n"
    "s_barrier\n"
    "s_waitcnt vmcnt(0)\n"
    "v_cmp_ne_u64 vcc, s[56:57], v[14:15]\n"
    "s_nop 1\n"
    "s_cmp_eq_u64 vcc, 0\n"
    "s_cbranch_scc1 .LG_h2\n"
    "s_mov_b32 s67, 100000\n"
    ".Lpoll15:\n"
    "v_mov_b32 v14, s56\n"
    "v_mov_b32 v15, s57\n"
    "s_mov_b32 exec_lo, 0x1010101\n"
    "s_mov_b32 exec_hi, 0x1010101\n"
    "v_mov_b32 v14, 0\n"
    "v_mov_b32 v15, 0\n"
    "buffer_atomic_or_x2 v[14:15], v19, s[28:31], s63 offen sc0 sc1\n"
    "s_mov_b64 exec, -1\n"
    "s_waitcnt vmcnt(0)\n"
    "v_cmp_ne_u64 vcc, s[56:57], v[14:15]\n"
    "s_nop 1\n"
    "s_cmp_eq_u64 vcc, 0\n"
    "s_cbranch_scc1 .Lpolld15\n"
    "s_sleep 1\n"
    "s_sub_u32 s67, s67, 1\n"
    "s_cbranch_scc0 .Lpoll15\n"
    ".Lpolld15:\n"
    ".LG_h2:\n"
    "buffer_load_dwordx4 v[128:131], v48, s[28:31], s61 offen offset:0 sc1\n"
    "buffer_load_dwordx4 v[132:135], v48, s[28:31], s61 offen offset:1024 sc1\n"
    "buffer_load_dwordx4 v[136:139], v49, s[28:31], s61 offen offset:0 sc1\n"
    "buffer_load_dwordx4 v[140:143], v49, s[28:31], s61 offen offset:1024 sc1\n"
    "buffer_load_dwordx4 v[144:147], v50, s[28:31], s61 offen offset:0 sc1\n"
    "buffer_load_dwordx4 v[148:151], v50, s[28:31], s61 offen offset:1024 sc1\n"
    "buffer_load_dwordx4 v[152:155], v51, s[28:31], s61 offen offset:0 sc1\n"
    "buffer_load_dwordx4 v[156:159], v51, s[28:31], s61 offen offset:1024 sc1\n"
    "v_mfma_f32_32x32x16_bf16 v[192:207], v[160:163], v[56:59], v[192:207]\n"
    "ds_read_b64_tr_b16 v[112:113], v26 offset:57344\n"
    "ds_read_b64_tr_b16 v[114:115], v26 offset:57856\n"
    "ds_read_b64_tr_b16 v[116:117], v26 offset:58368\n"
    "ds_read_b64_tr_b16 v[118:119], v26 offset:58880\n"
    "v_mfma_f32_32x32x16_bf16 v[208:223], v[164:167], v[56:59], v[208:223]\n"
    "v_and_b32 v35, 0xffff0000, v121\n"
    "v_lshlrev_b32 v36, 16, v122\n"
    "v_and_b32 v37, 0xffff0000, v122\n"
    "v_lshlrev_b32 v38, 16, v123\n"
    "v_mfma_f32_32x32x16_bf16 v[224:239], v[160:163], v[60:63], v[224:239]\n"
    "v_and_b32 v39, 0xffff0000, v123\n"
    "v_lshlrev_b32 v40, 16, v124\n"
    "v_and_b32 v41, 0xffff0000, v124\n"
    "v_lshlrev_b32 v42, 16, v125\n"
    "v_mfma_f32_32x32x16_bf16 v[240:255], v[164:167], v[60:63], v[240:255]\n"
    "v_and_b32 v43, 0xffff0000, v125\n"
    "v_lshlrev_b32 v44, 16, v126\n"
    "v_and_b32 v45, 0xffff0000, v126\n"
    "v_lshlrev_b32 v46, 16, v127\n"
    "v_mfma_f32_32x32x16_bf16 v[192:207], v[168:171], v[64:67], v[192:207]\n"
    "ds_read_b64_tr_b16 v[56:57], v25 offset:0\n"
    "ds_read_b64_tr_b16 v[58:59], v25 offset:512\n"
    "ds_read_b64_tr_b16 v[60:61], v25 offset:1024\n"
    "ds_read_b64_tr_b16 v[62:63], v25 offset:1536\n"
    "v_mfma_f32_32x32x16_bf16 v[208:223], v[172:175], v[64:67], v[208:223]\n"
    "buffer_load_dwordx4 v[160:163], v48, s[28:31], s62 offen offset:0 sc1\n"
    "buffer_load_dwordx4 v[164:167], v48, s[28:31], s62 offen offset:1024 sc1\n"
    "v_mfma_f32_32x32x16_bf16 v[224:239], v[168:171], v[68:71], v[224:239]\n"
    "v_and_b32 v47, 0xffff0000, v127\n"
    "v_mfma_f32_32x32x16_bf16 v[240:255], v[172:175], v[68:71], v[240:255]\n"
    "v_mfma_f32_32x32x16_bf16 v[192:207], v[176:179], v[72:75], v[192:207]\n"
    "ds_read_b64_tr_b16 v[64:65], v25 offset:8192\n"
    "ds_read_b64_tr_b16 v[66:67], v25 offset:8704\n"
    "ds_read_b64_tr_b16 v[68:69], v25 offset:9216\n"
    "ds_read_b64_tr_b16 v[70:71], v25 offset:9728\n"
    "v_mfma_f32_32x32x16_bf16 v[208:223], v[180:183], v[72:75], v[208:223]\n"
    "buffer_load_dwordx4 v[168:171], v49, s[28:31], s62 offen offset:0 sc1\n"
    "buffer_load_dwordx4 v[172:175], v49, s[28:31], s62 offen offset:1024 sc1\n"
    "v_mfma_f32_32x32x16_bf16 v[224:239], v[176:179], v[76:79], v[224:239]\n"
    "v_mfma_f32_32x32x16_bf16 v[240:255], v[180:183], v[76:79], v[240:255]\n"
    "v_mfma_f32_32x32x16_bf16 v[192:207], v[184:187], v[80:83], v[192:207]\n"
    "ds_read_b64_tr_b16 v[72:73], v25 offset:16384\n"
    "ds_read_b64_tr_b16 v[74:75], v25 offset:16896\n"
    "ds_read_b64_tr_b16 v[76:77], v25 offset:17408\n"
    "ds_read_b64_tr_b16 v[78:79], v25 offset:17920\n"
    "v_mfma_f32_32x32x16_bf16 v[208:223], v[188:191], v[80:83], v[208:223]\n"
    "buffer_load_dwordx4 v[176:179], v50, s[28:31], s62 offen offset:0 sc1\n"
    "buffer_load_dwordx4 v[180:183], v50, s[28:31], s62 offen offset:1024 sc1\n"
    "v_mfma_f32_32x32x16_bf16 v[224:239], v[184:187], v[84:87], v[224:239]\n"
    "s_waitcnt vmcnt(10)\n"
    "v_max3_u32 v13, v128, v129, v130\n"
    "v_max3_u32 v15, v131, v132, v133\n"
    "v_max3_u32 v13, v13, v134, v135\n"
    "v_max3_u32 v15, v15, v136, v137\n"
    "v_max3_u32 v13, v13, v138, v139\n"
    "v_mfma_f32_32x32x16_bf16 v[240:255], v[188:191], v[84:87], v[240:255]\n"
    "v_max3_u32 v15, v15, v140, v141\n"
    "v_max3_u32 v13, v13, v142, v143\n"
    "v_max_u32 v13, v13, v15\n"
    "v_cmp_eq_u32 vcc, -1, v13\n"
    "buffer_load_dwordx4 v[184:187], v51, s[28:31], s62 offen offset:0 sc1\n"
    "buffer_load_dwordx4 v[188:191], v51, s[28:31], s62 offen offset:1024 sc1\n"
    "ds_read_b64_tr_b16 v[80:81], v25 offset:24576\n"
    "ds_read_b64_tr_b16 v[82:83], v25 offset:25088\n"
    "ds_read_b64_tr_b16 v[84:85], v25 offset:25600\n"
    "ds_read_b64_tr_b16 v[86:87], v25 offset:26112\n"
    "s_waitcnt lgkmcnt(4)\n"
    "s_cbranch_vccnz .LG_rt0\n"
    ".LG_ok0:\n"
    "v_mfma_f32_32x32x16_bf16 v[208:223], v[92:95], v[128:131], v[208:223]\n"
    "ds_write_b128 v12, v[224:227] offset:4096\n"
    "v_mfma_f32_32x32x16_bf16 v[192:207], v[88:91], v[128:131], v[192:207]\n"
    "ds_read_b64_tr_b16 v[88:89], v4 offset:0\n"
    "ds_read_b64_tr_b16 v[90:91], v4 offset:512\n"
    "ds_read_b64_tr_b16 v[92:93], v4 offset:1024\n"
    "ds_read_b64_tr_b16 v[94:95], v4 offset:1536\n"
    "v_mfma_f32_32x32x16_bf16 v[208:223], v[100:103], v[132:135], v[208:223]\n"
    "ds_write_b128 v12, v[228:231] offset:5120\n"
    "v_mfma_f32_32x32x16_bf16 v[192:207], v[96:99], v[132:135], v[192:207]\n"
    "ds_read_b64_tr_b16 v[96:97], v4 offset:8192\n"
    "ds_read_b64_tr_b16 v[98:99], v4 offset:8704\n"
    "ds_read_b64_tr_b16 v[100:101], v4 offset:9216\n"
    "ds_read_b64_tr_b16 v[102:103], v4 offset:9728\n"
    "v_mfma_f32_32x32x16_bf16 v[208:223], v[108:111], v[136:139], v[208:223]\n"
    "ds_write_b128 v12, v[232:235] offset:6144\n"
    "s_waitcnt vmcnt(8)\n"
    "v_max3_u32 v13, v144, v145, v146\n"
    "v_max3_u32 v15, v147, v148, v149\n"
    "v_max3_u32 v13, v13, v150, v151\n"
    "v_mfma_f32_32x32x16_bf16 v[192:207], v[104:107], v[136:139], v[192:207]\n"
    "ds_read_b64_tr_b16 v[104:105], v4 offset:16384\n"
    "ds_read_b64_tr_b16 v[106:107], v4 offset:16896\n"
    "ds_read_b64_tr_b16 v[108:109], v4 offset:17408\n"
    "ds_read_b64_tr_b16 v[110:111], v4 offset:17920\n"
    "v_max3_u32 v15, v15, v152, v153\n"
    "v_max3_u32 v13, v13, v154, v155\n"
    "v_max3_u32 v15, v15, v156, v157\n"
    "v_mfma_f32_32x32x16_bf16 v[208:223], v[116:119], v[140:143], v[208:223]\n"
    "ds_write_b128 v12, v[236:239] offset:7168\n"
    "v_max3_u32 v13, v13, v158, v159\n"
    "v_max_u32 v13, v13, v15\n"
    "v_cmp_eq_u32 vcc, -1, v13\n"
    "v_mfma_f32_32x32x16_bf16 v[192:207], v[112:115], v[140:143], v[192:207]\n"
    "ds_read_b64_tr_b16 v[112:113], v2 offset:57344\n"
    "ds_read_b64_tr_b16 v[114:115], v2 offset:57856\n"
    "ds_read_b64_tr_b16 v[116:117], v2 offset:58368\n"
    "ds_read_b64_tr_b16 v[118:119], v2 offset:58880\n"
    "s_cbranch_vccnz .LG_rt4\n"
    ".LG_ok4:\n"
    "v_mfma_f32_32x32x16_bf16 v[208:223], v[60:63], v[144:147], v[208:223]\n"
    "ds_write_b128 v12, v[240:243] offset:8192\n"
    "v_mfma_f32_32x32x16_bf16 v[192:207], v[56:59], v[144:147], v[192:207]\n"
    "ds_read_b64_tr_b16 v[56:57], v3 offset:0\n"
    "ds_read_b64_tr_b16 v[58:59], v3 offset:512\n"
    "ds_read_b64_tr_b16 v[60:61], v3 offset:1024\n"
    "ds_read_b64_tr_b16 v[62:63], v3 offset:1536\n"
    "v_mfma_f32_32x32x16_bf16 v[208:223], v[68:71], v[148:151], v[208:223]\n"
    "ds_write_b128 v12, v[244:247] offset:9216\n"
    "v_mfma_f32_32x32x16_bf16 v[192:207], v[64:67], v[148:151], v[192:207]\n"
    "ds_read_b64_tr_b16 v[64:65], v3 offset:8192\n"
    "ds_read_b64_tr_b16 v[66:67], v3 offset:8704\n"
    "ds_read_b64_tr_b16 v[68:69], v3 offset:9216\n"
    "ds_read_b64_tr_b16 v[70:71], v3 offset:9728\n"
    "v_mfma_f32_32x32x16_bf16 v[208:223], v[76:79], v[152:155], v[208:223]\n"
    "ds_write_b128 v12, v[248:251] offset:10240\n"
    "s_waitcnt vmcnt(4)\n"
    "v_max3_u32 v13, v160, v161, v162\n"
    "v_max3_u32 v15, v163, v164, v165\n"
    "v_max3_u32 v13, v13, v166, v167\n"
    "v_mfma_f32_32x32x16_bf16 v[192:207], v[72:75], v[152:155], v[192:207]\n"
    "ds_read_b64_tr_b16 v[72:73], v3 offset:16384\n"
    "ds_read_b64_tr_b16 v[74:75], v3 offset:16896\n"
    "ds_read_b64_tr_b16 v[76:77], v3 offset:17408\n"
    "ds_read_b64_tr_b16 v[78:79], v3 offset:17920\n"
    "v_max3_u32 v15, v15, v168, v169\n"
    "v_max3_u32 v13, v13, v170, v171\n"
    "v_max3_u32 v15, v15, v172, v173\n"
    "s_waitcnt lgkmcnt(5)\n"
    "v_mfma_f32_32x32x16_bf16 v[208:223], v[84:87], v[156:159], v[208:223]\n"
    "ds_write_b128 v12, v[252:255] offset:11264\n"
    "v_max3_u32 v13, v13, v174, v175\n"
    "v_max_u32 v13, v13, v15\n"
    "v_cmp_eq_u32 vcc, -1, v13\n"
    "v_mfma_f32_32x32x16_bf16 v[192:207], v[80:83], v[156:159], v[192:207]\n"
    "ds_read_b64_tr_b16 v[80:81], v3 offset:24576\n"
    "ds_read_b64_tr_b16 v[82:83], v3 offset:25088\n"
    "ds_read_b64_tr_b16 v[84:85], v3 offset:25600\n"
    "ds_read_b64_tr_b16 v[86:87], v3 offset:26112\n"
    "s_waitcnt lgkmcnt(4)\n"
    "s_barrier\n"
    "ds_read_b128 v[224:227], v16 offset:4096\n"
    "ds_read_b128 v[228:231], v16 offset:5120\n"
    "ds_read_b128 v[232:235], v16 offset:6144\n"
    "ds_read_b128 v[236:239], v16 offset:7168\n"
    "s_cbranch_vccnz .LG_rt8\n"
    ".LG_ok8:\n"
    "v_mfma_f32_32x32x16_bf16 v[208:223], v[92:95], v[160:163], v[208:223]\n"
    "s_waitcnt vmcnt(0)\n"
    "v_max3_u32 v13, v176, v177, v178\n"
    "v_max3_u32 v15, v179, v180, v181\n"
    "v_max3_u32 v13, v13, v182, v183\n"
    "ds_read_b128 v[240:243], v18 offset:8192\n"
    "ds_read_b128 v[244:247], v18 offset:9216\n"
    "ds_read_b128 v[248:251], v18 offset:10240\n"
    "ds_read_b128 v[252:255], v18 offset:11264\n"
    "v_mfma_f32_32x32x16_bf16 v[208:223], v[100:103], v[164:167], v[208:223]\n"
    "v_max3_u32 v15, v15, v184, v185\n"
    "v_max3_u32 v13, v13, v186, v187\n"
    "v_max3_u32 v15, v15, v188, v189\n"
    "v_mfma_f32_32x32x16_bf16 v[208:223], v[108:111], v[168:171], v[208:223]\n"
    "v_max3_u32 v13, v13, v190, v191\n"
    "v_max_u32 v13, v13, v15\n"
    "v_cmp_eq_u32 vcc, -1, v13\n"
    "v_mfma_f32_32x32x16_bf16 v[208:223], v[116:119], v[172:175], v[208:223]\n"
    "s_cbranch_vccnz .LG_rt12\n"
    ".LG_ok12:\n"
    "v_mfma_f32_32x32x16_bf16 v[208:223], v[60:63], v[176:179], v[208:223]\n"
    "s_waitcnt lgkmcnt(0)\n"
    "v_fmac_f32 v32, v31, v224\n"
    "v_fmac_f32 v33, v31, v225\n"
    "v_fmac_f32 v34, v31, v226\n"
    "v_fmac_f32 v35, v31, v227\n"
    "v_fmac_f32 v36, v31, v228\n"
    "v_fmac_f32 v37, v31, v229\n"
    "v_mfma_f32_32x32x16_bf16 v[208:223], v[68:71], v[180:183], v[208:223]\n"
    "v_fmac_f32 v38, v31, v230\n"
    "v_fmac_f32 v39, v31, v231\n"
    "v_fmac_f32 v40, v31, v232\n"
    "v_fmac_f32 v41, v31, v233\n"
    "v_fmac_f32 v42, v31, v234\n"
    "v_fmac_f32 v43, v31, v235\n"
    "v_mfma_f32_32x32x16_bf16 v[208:223], v[76:79], v[184:187], v[208:223]\n"
    "v_fmac_f32 v44, v31, v236\n"
    "v_fmac_f32 v45, v31, v237\n"
    "v_fmac_f32 v46, v31, v238\n"
    "v_fmac_f32 v47, v31, v239\n"
    "v_fmac_f32 v32, v31, v240\n"
    "v_fmac_f32 v33, v31, v241\n"
    "v_mfma_f32_32x32x16_bf16 v[208:223], v[84:87], v[188:191], v[208:223]\n"
    "v_fmac_f32 v34, v31, v242\n"
    "v_fmac_f32 v35, v31, v243\n"
    "v_fmac_f32 v36, v31, v244\n"
    "v_fmac_f32 v37, v31, v245\n"
    "v_fmac_f32 v38, v31, v246\n"
    "v_fmac_f32 v39, v31, v247\n"
    "v_mfma_f32_32x32x16_bf16 v[192:207], v[88:91], v[160:163], v[192:207]\n"
    "v_fmac_f32 v40, v31, v248\n"
    "v_fmac_f32 v41, v31, v249\n"
    "v_fmac_f32 v42, v31, v250\n"
    "v_fmac_f32 v43, v31, v251\n"
    "v_fmac_f32 v44, v31, v252\n"
    "v_fmac_f32 v45, v31, v253\n"
    "v_mfma_f32_32x32x16_bf16 v[192:207], v[96:99], v[164:167], v[192:207]\n"
    "v_fmac_f32 v46, v31, v254\n"
    "v_fmac_f32 v47, v31, v255\n"
    "v_mfma_f32_32x32x16_bf16 v[192:207], v[104:107], v[168:171], v[192:207]\n"
    "ds_write_b128 v12, v[208:211] offset:0\n"
    "ds_write_b128 v12, v[212:215] offset:1024\n"
    "v_mfma_f32_32x32x16_bf16 v[192:207], v[112:115], v[172:175], v[192:207]\n"
    "ds_write_b128 v12, v[216:219] offset:2048\n"
    "ds_write_b128 v12, v[220:223] offset:3072\n"
    "v_mfma_f32_32x32x16_bf16 v[192:207], v[56:59], v[176:179], v[192:207]\n"
    "v_mfma_f32_32x32x16_bf16 v[192:207], v[64:67], v[180:183], v[192:207]\n"
    "s_waitcnt lgkmcnt(0)\n"
    "s_barrier\n"
    "ds_read_b128 v[208:211], v17 offset:0\n"
    "ds_read_b128 v[212:215], v17 offset:1024\n"
    "ds_read_b128 v[216:219], v17 offset:2048\n"
    "ds_read_b128 v[220:223], v17 offset:3072\n"
    "v_mfma_f32_32x32x16_bf16 v[192:207], v[72:75], v[184:187], v[192:207]\n"
    "v_mfma_f32_32x32x16_bf16 v[192:207], v[80:83], v[188:191], v[192:207]\n"
    "s_waitcnt lgkmcnt(0)\n"
    "v_fmac_f32 v32, v31, v208\n"
    "v_fmac_f32 v33, v31, v209\n"
    "v_fmac_f32 v34, v31, v210\n"
    "v_fmac_f32 v35, v31, v211\n"
    "v_fmac_f32 v36, v31, v212\n"
    "v_fmac_f32 v37, v31, v213\n"
    "v_fmac_f32 v38, v31, v214\n"
    "v_fmac_f32 v39, v31, v215\n"
    "v_fmac_f32 v40, v31, v216\n"
    "v_fmac_f32 v41, v31, v217\n"
    "v_fmac_f32 v42, v31, v218\n"
    "v_fmac_f32 v43, v31, v219\n"
    "v_fmac_f32 v44, v31, v220\n"
    "v_fmac_f32 v45, v31, v221\n"
    "v_fmac_f32 v46, v31, v222\n"
    "v_fmac_f32 v47, v31, v223\n"
    "v_pk_fma_f32 v[32:33], v[192:193], v[30:31], v[32:33] op_sel:[0,1,0] op_sel_hi:[1,1,1]\n"
    "v_pk_fma_f32 v[34:35], v[194:195], v[30:31], v[34:35] op_sel:[0,1,0] op_sel_hi:[1,1,1]\n"
    "v_cvt_pk_bf16_f32 v56, v32, v33\n"
    "v_cvt_pk_bf16_f32 v57, v34, v35\n"
    "ds_write_b64 v52, v[56:57]\n"
    "v_pk_fma_f32 v[36:37], v[196:197], v[30:31], v[36:37] op_sel:[0,1,0] op_sel_hi:[1,1,1]\n"
    "v_pk_fma_f32 v[38:39], v[198:199], v[30:31], v[38:39] op_sel:[0,1,0] op_sel_hi:[1,1,1]\n"
    "v_cvt_pk_bf16_f32 v58, v36, v37\n"
    "v_cvt_pk_bf16_f32 v59, v38, v39\n"
    "ds_write_b64 v53, v[58:59]\n"
    "v_pk_fma_f32 v[40:41], v[200:201], v[30:31], v[40:41] op_sel:[0,1,0] op_sel_hi:[1,1,1]\n"
    "v_pk_fma_f32 v[42:43], v[202:203], v[30:31], v[42:43] op_sel:[0,1,0] op_sel_hi:[1,1,1]\n"
    "v_cvt_pk_bf16_f32 v60, v40, v41\n"
    "v_cvt_pk_bf16_f32 v61, v42, v43\n"
    "ds_write_b64 v54, v[60:61]\n"
    "v_pk_fma_f32 v[44:45], v[204:205], v[30:31], v[44:45] op_sel:[0,1,0] op_sel_hi:[1,1,1]\n"
    "v_pk_fma_f32 v[46:47], v[206:207], v[30:31], v[46:47] op_sel:[0,1,0] op_sel_hi:[1,1,1]\n"
    "v_cvt_pk_bf16_f32 v62, v44, v45\n"
    "v_cvt_pk_bf16_f32 v63, v46, v47\n"
    "ds_write_b64 v55, v[62:63]\n"
    "s_waitcnt lgkmcnt(0)\n"
    "s_barrier\n"
    "ds_read_b128 v[64:67], v30\n"
    "ds_read_b128 v[68:71], v30 offset:1024\n"
    "s_waitcnt lgkmcnt(1)\n"
    "buffer_store_dwordx4 v[64:67], v27, s[40:43], s91 offen sc1\n"
    "s_waitcnt lgkmcnt(0)\n"
    "buffer_store_dwordx4 v[68:71], v27, s[40:43], s74 offen sc1\n"
    "s_endpgm\n"
    ".LG_rt0:\n"
    "s_mov_b32 s67, 100000\n"
    ".LG_rl0:\n"
    "s_sleep 2\n"
    "buffer_load_dwordx4 v[128:131], v48, s[28:31], s61 offen offset:0 sc1\n"
    "buffer_load_dwordx4 v[132:135], v48, s[28:31], s61 offen offset:1024 sc1\n"
    "buffer_load_dwordx4 v[136:139], v49, s[28:31], s61 offen offset:0 sc1\n"
    "buffer_load_dwordx4 v[140:143], v49, s[28:31], s61 offen offset:1024 sc1\n"
    "s_waitcnt vmcnt(0)\n"
    "v_max3_u32 v13, v128, v129, v130\n"
    "v_max3_u32 v15, v131, v132, v133\n"
    "v_max3_u32 v13, v13, v134, v135\n"
    "v_max3_u32 v15, v15, v136, v137\n"
    "v_max3_u32 v13, v13, v138, v139\n"
    "v_max3_u32 v15, v15, v140, v141\n"
    "v_max3_u32 v13, v13, v142, v143\n"
    "v_max_u32 v13, v13, v15\n"
    "v_cmp_eq_u32 vcc, -1, v13\n"
    "s_nop 4\n"
    "s_cbranch_vccz .LG_ok0\n"
    "s_sub_u32 s67, s67, 1\n"
    "s_cbranch_scc0 .LG_rl0\n"
    "s_branch .LG_ok0\n"
    ".LG_rt4:\n"
    "s_mov_b32 s67, 100000\n"
    ".LG_rl4:\n"
    "s_sleep 2\n"
    "buffer_load_dwordx4 v[144:147], v50, s[28:31], s61 offen offset:0 sc1\n"
    "buffer_load_dwordx4 v[148:151], v50, s[28:31], s61 offen offset:1024 sc1\n"
    "buffer_load_dwordx4 v[152:155], v51, s[28:31], s61 offen offset:0 sc1\n"
    "buffer_load_dwordx4 v[156:159], v51, s[28:31], s61 offen offset:1024 sc1\n"
    "s_waitcnt vmcnt(0)\n"
    "v_max3_u32 v13, v144, v145, v146\n"
    "v_max3_u32 v15, v147, v148, v149\n"
    "v_max3_u32 v13, v13, v150, v151\n"
    "v_max3_u32 v15, v15, v152, v153\n"
    "v_max3_u32 v13, v13, v154, v155\n"
    "v_max3_u32 v15, v15, v156, v157\n"
    "v_max3_u32 v13, v13, v158, v159\n"
    "v_max_u32 v13, v13, v15\n"
    "v_cmp_eq_u32 vcc, -1, v13\n"
    "s_nop 4\n"
    "s_cbranch_vccz .LG_ok4\n"
    "s_sub_u32 s67, s67, 1\n"
    "s_cbranch_scc0 .LG_rl4\n"
    "s_branch .LG_ok4\n"
    ".LG_rt8:\n"
    "s_mov_b32 s67, 100000\n"
    ".LG_rl8:\n"
    "s_sleep 2\n"
    "buffer_load_dwordx4 v[160:163], v48, s[28:31], s62 offen offset:0 sc1\n"
    "buffer_load_dwordx4 v[164:167], v48, s[28:31], s62 offen offset:1024 sc1\n"
    "buffer_load_dwordx4 v[168:171], v49, s[28:31], s62 offen offset:0 sc1\n"
    "buffer_load_dwordx4 v[172:175], v49, s[28:31], s62 offen offset:1024 sc1\n"
    "s_waitcnt vmcnt(0)\n"
    "v_max3_u32 v13, v160, v161, v162\n"
    "v_max3_u32 v15, v163, v164, v165\n"
    "v_max3_u32 v13, v13, v166, v167\n"
    "v_max3_u32 v15, v15, v168, v169\n"
    "v_max3_u32 v13, v13, v170, v171\n"
    "v_max3_u32 v15, v15, v172, v173\n"
    "v_max3_u32 v13, v13, v174, v175\n"
    "v_max_u32 v13, v13, v15\n"
    "v_cmp_eq_u32 vcc, -1, v13\n"
    "s_nop 4\n"
    "s_cbranch_vccz .LG_ok8\n"
    "s_sub_u32 s67, s67, 1\n"
    "s_cbranch_scc0 .LG_rl8\n"
    "s_branch .LG_ok8\n"
    ".LG_rt12:\n"
    "s_mov_b32 s67, 100000\n"
    ".LG_rl12:\n"
    "s_sleep 2\n"
    "buffer_load_dwordx4 v[176:179], v50, s[28:31], s62 offen offset:0 sc1\n"
    "buffer_load_dwordx4 v[180:183], v50, s[28:31], s62 offen offset:1024 sc1\n"
    "buffer_load_dwordx4 v[184:187], v51, s[28:31], s62 offen offset:0 sc1\n"
    "buffer_load_dwordx4 v[188:191], v51, s[28:31], s62 offen offset:1024 sc1\n"
    "s_waitcnt vmcnt(0)\n"
    "v_max3_u32 v13, v176, v177, v178\n"
    "v_max3_u32 v15, v179, v180, v181\n"
    "v_max3_u32 v13, v13, v182, v183\n"
    "v_max3_u32 v15, v15, v184, v185\n"
    "v_max3_u32 v13, v13, v186, v187\n"
    "v_max3_u32 v15, v15, v188, v189\n"
    "v_max3_u32 v13, v13, v190, v191\n"
    "v_max_u32 v13, v13, v15\n"
    "v_cmp_eq_u32 vcc, -1, v13\n"
    "s_nop 4\n"
    "s_cbranch_vccz .LG_ok12\n"
    "s_sub_u32 s67, s67, 1\n"
    "s_cbranch_scc0 .LG_rl12\n"
    "s_branch .LG_ok12\n"
    ".LG_cs:\n"
    "s_mov_b32 s67, 100000\n"
    ".LG_cs1:\n"
    "s_sleep 2\n"
    "buffer_load_dwordx2 v[60:61], v14, s[28:31], s90 offen sc1\n"
    "buffer_load_dwordx2 v[116:117], v14, s[28:31], s90 offen offset:512 sc1\n"
    "s_waitcnt vmcnt(0)\n"
    "v_cmp_ne_u32 vcc, s56, v61\n"
    "v_cmp_ne_u32 s[60:61], s56, v117\n"
    "s_nop 1\n"
    "s_or_b64 vcc, vcc, s[60:61]\n"
    "s_cbranch_scc0 .LG_csd\n"
    "s_sub_u32 s67, s67, 1\n"
    "s_cbranch_scc0 .LG_cs1\n"
    "s_branch .LG_csd\n"
    ".LA:\n"
    "s_cmp_lg_u32 s52, 0\n"
    "s_cbranch_scc1 .LA_noct\n"
    "s_getpc_b64 s[72:73]\n"
    ".Lpc:\n"
    "s_sub_u32 s72, s72, .Lpc-.Ltop\n"
    "s_subb_u32 s73, s73, 0\n"
    "s_lshr_b32 s59, s24, 3\n"
    "s_and_b32 s59, s59, 7\n"
    "s_lshl_b32 s59, s59, 13\n"
    "v_lshlrev_b32 v12, 7, v1\n"
    "v_add_u32 v12, s59, v12\n"
    "v_min_u32 v12, .Lend-.Ltop-4, v12\n"
    "global_load_dword v127, v12, s[72:73]\n"
    ".LA_noct:\n"
    "v_and_b32 v6, 31, v1\n"
    "v_lshrrev_b32 v7, 5, v1\n"
    "v_bfe_u32 v8, v1, 2, 2\n"
    "v_and_b32 v9, 3, v1\n"
    "v_lshlrev_b32 v9, 3, v9\n"
    "v_bfe_u32 v11, v1, 4, 1\n"
    "v_lshl_add_u32 v9, v11, 5, v9\n"
    "v_bfe_u32 v12, v6, 1, 3\n"
    "v_xor_b32 v12, v12, v7\n"
    "v_xor_b32 v13, 0, v12\n"
    "v_lshlrev_b32 v13, 4, v13\n"
    "v_lshl_add_u32 v112, v6, 7, v13\n"
    "v_xor_b32 v13, 2, v12\n"
    "v_lshlrev_b32 v13, 4, v13\n"
    "v_lshl_add_u32 v113, v6, 7, v13\n"
    "v_xor_b32 v13, 4, v12\n"
    "v_lshlrev_b32 v13, 4, v13\n"
    "v_lshl_add_u32 v114, v6, 7, v13\n"
    "v_xor_b32 v13, 6, v12\n"
    "v_lshlrev_b32 v13, 4, v13\n"
    "v_lshl_add_u32 v115, v6, 7, v13\n"
    "v_mov_b32 v15, 0x3f803f80\n"
    "s_waitcnt lgkmcnt(0)\n"
    "s_lshr_b32 s59, s52, 1\n"
    "s_lshr_b32 s60, s54, 3\n"
    "s_cmp_eq_u32 s59, s60\n"
    "s_cselect_b32 s66, s65, 0\n"
    "s_lshl_b32 s59, s52, 12\n"
    "v_add_u32 v112, s59, v112\n"
    "v_add_u32 v113, s59, v113\n"
    "v_add_u32 v114, s59, v114\n"
    "v_add_u32 v115, s59, v115\n"
    "v_mov_b32 v3, 0xdddddddd\n"
    "v_and_b32 v12, 27, v6\n"
    "v_cmp_eq_u32 vcc, 16, v12\n"
    "v_mov_b32 v12, 0x7fff0000\n"
    "v_mov_b32 v13, 0x200\n"
    "v_cndmask_b32 v12, v12, v13, vcc\n"
    "v_cmp_gt_u32 vcc, 16, v6\n"
    "v_lshlrev_b32 v2, 4, v6\n"
    "v_lshl_add_u32 v2, v7, 8, v2\n"
    "v_cndmask_b32 v2, v12, v2, vcc\n"
    "v_mov_b32 v52, 0\n"
    "v_mov_b32 v53, 0\n"
    "v_mov_b32 v54, 0\n"
    "v_mov_b32 v55, 0\n"
    "s_mov_b32 s28, s22\n"
    "s_and_b32 s29, s23, 0xffff\n"
    "s_mov_b32 s30, 10485760\n"
    "s_mov_b32 s31, 0x20000\n"
    "buffer_load_dwordx2 v[4:5], off, s[28:31], 0 sc1\n"
    "s_nop 1\n"
    "v_mfma_f32_32x32x16_bf16 v[160:175], v[52:55], v[52:55], 0\n"
    "v_mfma_f32_32x32x16_bf16 v[176:191], v[52:55], v[52:55], 0\n"
    "v_mfma_f32_32x32x16_bf16 v[192:207], v[52:55], v[52:55], 0\n"
    "v_mfma_f32_32x32x16_bf16 v[208:223], v[52:55], v[52:55], 0\n"
    "v_mfma_f32_32x32x16_bf16 v[224:239], v[52:55], v[52:55], 0\n"
    "v_mfma_f32_32x32x16_bf16 v[240:255], v[52:55], v[52:55], 0\n"
    "v_mov_b32 v41, 0\n"
    "v_mov_b32 v42, 0\n"
    "v_mov_b32 v43, 0\n"
    "v_mov_b32 v45, 0\n"
    "v_mov_b32 v46, 0\n"
    "v_mov_b32 v47, 0\n"
    "s_cmp_eq_u32 s65, 0\n"
    "s_cbranch_scc1 .LA_gen\n"
    "s_cmp_eq_u32 s66, 0\n"
    "s_cbranch_scc1 .LA_help\n"
    "v_lshl_add_u32 v12, v7, 3, v8\n"
    "v_lshlrev_b32 v12, 5, v12\n"
    "v_lshl_add_u32 v12, v11, 11, v12\n"
    "v_and_b32 v13, 3, v1\n"
    "v_lshl_add_u32 v12, v13, 3, v12\n"
    "v_add_u32 v12, 131072, v12\n"
    "s_lshl_b32 s59, s52, 4\n"
    "s_add_u32 s59, s59, s53\n"
    "s_mul_i32 s60, s59, 2304\n"
    "s_add_u32 s60, s60, 65536\n"
    "s_lshl_b32 s62, s59, 12\n"
    "s_add_u32 s62, s62, 262144\n"
    "s_lshl_b32 s59, s59, 7\n"
    "s_add_u32 s59, s59, 20480\n"
    "s_xor_b32 s61, s52, 2\n"
    "s_lshl_b32 s61, s61, 12\n"
    "s_add_u32 s61, s61, 114688\n"
    "v_lshrrev_b32 v13, 4, v1\n"
    "v_lshl_add_u32 v13, v13, 3, v8\n"
    "v_lshlrev_b32 v13, 5, v13\n"
    "v_and_b32 v14, 3, v1\n"
    "v_lshl_add_u32 v13, v14, 3, v13\n"
    "v_add_u32 v13, s61, v13\n"
    "v_lshlrev_b32 v14, 5, v6\n"
    "v_lshl_add_u32 v14, v7, 3, v14\n"
    "v_add_u32 v14, s61, v14\n"
    "v_lshlrev_b32 v27, 2, v6\n"
    "v_add_u32 v27, s61, v27\n"
    "v_mov_b32 v120, v15\n"
    "v_mov_b32 v121, v15\n"
    "v_mov_b32 v122, v15\n"
    "v_mov_b32 v123, v15\n"
    "v_bfe_u32 v118, v1, 3, 1\n"
    "v_lshl_add_u32 v118, v118, 9, v10\n"
    "v_lshl_add_u32 v119, v7, 7, v10\n"
    "v_mov_b32 v116, 0\n"
    "v_mov_b32 v117, 0\n"
    "s_barrier\n"
    "ds_read_b64_tr_b16 v[128:129], v12 offset:0\n"
    "ds_read_b64_tr_b16 v[130:131], v12 offset:128\n"
    "ds_read_b64_tr_b16 v[132:133], v12 offset:512\n"
    "ds_read_b64_tr_b16 v[134:135], v12 offset:640\n"
    "ds_read_b64_tr_b16 v[136:137], v12 offset:1024\n"
    "ds_read_b64_tr_b16 v[138:139], v12 offset:1152\n"
    "ds_read_b64_tr_b16 v[140:141], v12 offset:1536\n"
    "ds_read_b64_tr_b16 v[142:143], v12 offset:1664\n"
    "ds_read_b128 v[64:67], v112 offset:0\n"
    "ds_read_b128 v[68:71], v113 offset:0\n"
    "ds_read_b128 v[72:75], v114 offset:0\n"
    "ds_read_b128 v[76:79], v115 offset:0\n"
    "s_waitcnt lgkmcnt(4)\n"
    "v_dot2c_f32_bf16 v116, v128, v15\n"
    "v_dot2c_f32_bf16 v117, v129, v15\n"
    "v_dot2c_f32_bf16 v116, v130, v15\n"
    "v_dot2c_f32_bf16 v117, v131, v15\n"
    "v_dot2c_f32_bf16 v116, v132, v15\n"
    "v_dot2c_f32_bf16 v117, v133, v15\n"
    "v_dot2c_f32_bf16 v116, v134, v15\n"
    "v_dot2c_f32_bf16 v117, v135, v15\n"
    "v_dot2c_f32_bf16 v116, v136, v15\n"
    "v_dot2c_f32_bf16 v117, v137, v15\n"
    "v_dot2c_f32_bf16 v116, v138, v15\n"
    "v_dot2c_f32_bf16 v117, v139, v15\n"
    "v_dot2c_f32_bf16 v116, v140, v15\n"
    "v_dot2c_f32_bf16 v117, v141, v15\n"
    "v_dot2c_f32_bf16 v116, v142, v15\n"
    "v_dot2c_f32_bf16 v117, v143, v15\n"
    "s_waitcnt lgkmcnt(0)\n"
    "v_mfma_f32_32x32x16_bf16 v[96:111], v[128:131], v[64:67], 0\n"
    "v_mfma_f32_32x32x16_bf16 v[96:111], v[132:135], v[68:71], v[96:111]\n"
    "s_barrier\n"
    "ds_read_b64_tr_b16 v[144:145], v12 offset:4096\n"
    "ds_read_b64_tr_b16 v[146:147], v12 offset:4224\n"
    "ds_read_b64_tr_b16 v[148:149], v12 offset:4608\n"
    "ds_read_b64_tr_b16 v[150:151], v12 offset:4736\n"
    "ds_read_b64_tr_b16 v[152:153], v12 offset:5120\n"
    "ds_read_b64_tr_b16 v[154:155], v12 offset:5248\n"
    "ds_read_b64_tr_b16 v[156:157], v12 offset:5632\n"
    "ds_read_b64_tr_b16 v[158:159], v12 offset:5760\n"
    "ds_read_b128 v[80:83], v112 offset:16384\n"
    "ds_read_b128 v[84:87], v113 offset:16384\n"
    "ds_read_b128 v[88:91], v114 offset:16384\n"
    "ds_read_b128 v[92:95], v115 offset:16384\n"
    "v_mfma_f32_32x32x16_bf16 v[96:111], v[136:139], v[72:75], v[96:111]\n"
    "v_mfma_f32_32x32x16_bf16 v[96:111], v[140:143], v[76:79], v[96:111]\n"
    "s_waitcnt lgkmcnt(4)\n"
    "v_dot2c_f32_bf16 v116, v144, v15\n"
    "v_dot2c_f32_bf16 v117, v145, v15\n"
    "v_dot2c_f32_bf16 v116, v146, v15\n"
    "v_dot2c_f32_bf16 v117, v147, v15\n"
    "v_dot2c_f32_bf16 v116, v148, v15\n"
    "v_dot2c_f32_bf16 v117, v149, v15\n"
    "v_dot2c_f32_bf16 v116, v150, v15\n"
    "v_dot2c_f32_bf16 v117, v151, v15\n"
    "v_dot2c_f32_bf16 v116, v152, v15\n"
    "v_dot2c_f32_bf16 v117, v153, v15\n"
    "v_dot2c_f32_bf16 v116, v154, v15\n"
    "v_dot2c_f32_bf16 v117, v155, v15\n"
    "v_dot2c_f32_bf16 v116, v156, v15\n"
    "v_dot2c_f32_bf16 v117, v157, v15\n"
    "v_dot2c_f32_bf16 v116, v158, v15\n"
    "v_dot2c_f32_bf16 v117, v159, v15\n"
    "s_waitcnt lgkmcnt(0)\n"
    "v_mfma_f32_32x32x16_bf16 v[96:111], v[144:147], v[80:83], v[96:111]\n"
    "v_mfma_f32_32x32x16_bf16 v[96:111], v[148:151], v[84:87], v[96:111]\n"
    "s_barrier\n"
    "ds_read_b64_tr_b16 v[128:129], v12 offset:8192\n"
    "ds_read_b64_tr_b16 v[130:131], v12 offset:8320\n"
    "ds_read_b64_tr_b16 v[132:133], v12 offset:8704\n"
    "ds_read_b64_tr_b16 v[134:135], v12 offset:8832\n"
    "ds_read_b64_tr_b16 v[136:137], v12 offset:9216\n"
    "ds_read_b64_tr_b16 v[138:139], v12 offset:9344\n"
    "ds_read_b64_tr_b16 v[140:141], v12 offset:9728\n"
    "ds_read_b64_tr_b16 v[142:143], v12 offset:9856\n"
    "ds_read_b128 v[64:67], v112 offset:32768\n"
    "ds_read_b128 v[68:71], v113 offset:32768\n"
    "ds_read_b128 v[72:75], v114 offset:32768\n"
    "ds_read_b128 v[76:79], v115 offset:32768\n"
    "v_mfma_f32_32x32x16_bf16 v[96:111], v[152:155], v[88:91], v[96:111]\n"
    "v_mfma_f32_32x32x16_bf16 v[96:111], v[156:159], v[92:95], v[96:111]\n"
    "s_waitcnt lgkmcnt(4)\n"
    "v_dot2c_f32_bf16 v116, v128, v15\n"
    "v_dot2c_f32_bf16 v117, v129, v15\n"
    "v_dot2c_f32_bf16 v116, v130, v15\n"
    "v_dot2c_f32_bf16 v117, v131, v15\n"
    "v_dot2c_f32_bf16 v116, v132, v15\n"
    "v_dot2c_f32_bf16 v117, v133, v15\n"
    "v_dot2c_f32_bf16 v116, v134, v15\n"
    "v_dot2c_f32_bf16 v117, v135, v15\n"
    "v_dot2c_f32_bf16 v116, v136, v15\n"
    "v_dot2c_f32_bf16 v117, v137, v15\n"
    "v_dot2c_f32_bf16 v116, v138, v15\n"
    "v_dot2c_f32_bf16 v117, v139, v15\n"
    "v_dot2c_f32_bf16 v116, v140, v15\n"
    "v_dot2c_f32_bf16 v117, v141, v15\n"
    "v_dot2c_f32_bf16 v116, v142, v15\n"
    "v_dot2c_f32_bf16 v117, v143, v15\n"
    "s_waitcnt lgkmcnt(0)\n"
    "v_mfma_f32_32x32x16_bf16 v[96:111], v[128:131], v[64:67], v[96:111]\n"
    "v_mfma_f32_32x32x16_bf16 v[96:111], v[132:135], v[68:71], v[96:111]\n"
    "s_barrier\n"
    "ds_read_b64_tr_b16 v[144:145], v12 offset:12288\n"
    "ds_read_b64_tr_b16 v[146:147], v12 offset:12416\n"
    "ds_read_b64_tr_b16 v[148:149], v12 offset:12800\n"
    "ds_read_b64_tr_b16 v[150:151], v12 offset:12928\n"
    "ds_read_b64_tr_b16 v[152:153], v12 offset:13312\n"
    "ds_read_b64_tr_b16 v[154:155], v12 offset:13440\n"
    "ds_read_b64_tr_b16 v[156:157], v12 offset:13824\n"
    "ds_read_b64_tr_b16 v[158:159], v12 offset:13952\n"
    "ds_read_b128 v[80:83], v112 offset:49152\n"
    "ds_read_b128 v[84:87], v113 offset:49152\n"
    "ds_read_b128 v[88:91], v114 offset:49152\n"
    "ds_read_b128 v[92:95], v115 offset:49152\n"
    "v_mfma_f32_32x32x16_bf16 v[96:111], v[136:139], v[72:75], v[96:111]\n"
    "v_mfma_f32_32x32x16_bf16 v[96:111], v[140:143], v[76:79], v[96:111]\n"
    "s_waitcnt lgkmcnt(4)\n"
    "v_dot2c_f32_bf16 v116, v144, v15\n"
    "v_dot2c_f32_bf16 v117, v145, v15\n"
    "v_dot2c_f32_bf16 v116, v146, v15\n"
    "v_dot2c_f32_bf16 v117, v147, v15\n"
    "v_dot2c_f32_bf16 v116, v148, v15\n"
    "v_dot2c_f32_bf16 v117, v149, v15\n"
    "v_dot2c_f32_bf16 v116, v150, v15\n"
    "v_dot2c_f32_bf16 v117, v151, v15\n"
    "v_dot2c_f32_bf16 v116, v152, v15\n"
    "v_dot2c_f32_bf16 v117, v153, v15\n"
    "v_dot2c_f32_bf16 v116, v154, v15\n"
    "v_dot2c_f32_bf16 v117, v155, v15\n"
    "v_dot2c_f32_bf16 v116, v156, v15\n"
    "v_dot2c_f32_bf16 v117, v157, v15\n"
    "v_dot2c_f32_bf16 v116, v158, v15\n"
    "v_dot2c_f32_bf16 v117, v159, v15\n"
    "s_waitcnt lgkmcnt(0)\n"
    "v_mfma_f32_32x32x16_bf16 v[96:111], v[144:147], v[80:83], v[96:111]\n"
    "v_mfma_f32_32x32x16_bf16 v[96:111], v[148:151], v[84:87], v[96:111]\n"
    "s_barrier\n"
    "v_add_u32 v112, 0x10000, v112\n"
    "v_add_u32 v113, 0x10000, v113\n"
    "v_add_u32 v114, 0x10000, v114\n"
    "v_add_u32 v115, 0x10000, v115\n"
    "ds_read_b64_tr_b16 v[128:129], v12 offset:16384\n"
    "ds_read_b64_tr_b16 v[130:131], v12 offset:16512\n"
    "ds_read_b64_tr_b16 v[132:133], v12 offset:16896\n"
    "ds_read_b64_tr_b16 v[134:135], v12 offset:17024\n"
    "ds_read_b64_tr_b16 v[136:137], v12 offset:17408\n"
    "ds_read_b64_tr_b16 v[138:139], v12 offset:17536\n"
    "ds_read_b64_tr_b16 v[140:141], v12 offset:17920\n"
    "ds_read_b64_tr_b16 v[142:143], v12 offset:18048\n"
    "ds_read_b128 v[64:67], v112 offset:0\n"
    "ds_read_b128 v[68:71], v113 offset:0\n"
    "ds_read_b128 v[72:75], v114 offset:0\n"
    "ds_read_b128 v[76:79], v115 offset:0\n"
    "v_mfma_f32_32x32x16_bf16 v[96:111], v[152:155], v[88:91], v[96:111]\n"
    "v_mfma_f32_32x32x16_bf16 v[96:111], v[156:159], v[92:95], v[96:111]\n"
    "s_waitcnt lgkmcnt(4)\n"
    "v_dot2c_f32_bf16 v116, v128, v15\n"
    "v_dot2c_f32_bf16 v117, v129, v15\n"
    "v_dot2c_f32_bf16 v116, v130, v15\n"
    "v_dot2c_f32_bf16 v117, v131, v15\n"
    "v_dot2c_f32_bf16 v116, v132, v15\n"
    "v_dot2c_f32_bf16 v117, v133, v15\n"
    "v_dot2c_f32_bf16 v116, v134, v15\n"
    "v_dot2c_f32_bf16 v117, v135, v15\n"
    "v_dot2c_f32_bf16 v116, v136, v15\n"
    "v_dot2c_f32_bf16 v117, v137, v15\n"
    "v_dot2c_f32_bf16 v116, v138, v15\n"
    "v_dot2c_f32_bf16 v117, v139, v15\n"
    "v_dot2c_f32_bf16 v116, v140, v15\n"
    "v_dot2c_f32_bf16 v117, v141, v15\n"
    "v_dot2c_f32_bf16 v116, v142, v15\n"
    "v_dot2c_f32_bf16 v117, v143, v15\n"
    "s_waitcnt lgkmcnt(0)\n"
    "v_mfma_f32_32x32x16_bf16 v[96:111], v[128:131], v[64:67], v[96:111]\n"
    "v_mfma_f32_32x32x16_bf16 v[96:111], v[132:135], v[68:71], v[96:111]\n"
    "s_barrier\n"
    "ds_read_b64_tr_b16 v[144:145], v12 offset:20480\n"
    "ds_read_b64_tr_b16 v[146:147], v12 offset:20608\n"
    "ds_read_b64_tr_b16 v[148:149], v12 offset:20992\n"
    "ds_read_b64_tr_b16 v[150:151], v12 offset:21120\n"
    "ds_read_b64_tr_b16 v[152:153], v12 offset:21504\n"
    "ds_read_b64_tr_b16 v[154:155], v12 offset:21632\n"
    "ds_read_b64_tr_b16 v[156:157], v12 offset:22016\n"
    "ds_read_b64_tr_b16 v[158:159], v12 offset:22144\n"
    "ds_read_b128 v[80:83], v112 offset:16384\n"
    "ds_read_b128 v[84:87], v113 offset:16384\n"
    "ds_read_b128 v[88:91], v114 offset:16384\n"
    "ds_read_b128 v[92:95], v115 offset:16384\n"
    "v_mfma_f32_32x32x16_bf16 v[96:111], v[136:139], v[72:75], v[96:111]\n"
    "v_mfma_f32_32x32x16_bf16 v[96:111], v[140:143], v[76:79], v[96:111]\n"
    "s_waitcnt lgkmcnt(4)\n"
    "v_dot2c_f32_bf16 v116, v144, v15\n"
    "v_dot2c_f32_bf16 v117, v145, v15\n"
    "v_dot2c_f32_bf16 v116, v146, v15\n"
    "v_dot2c_f32_bf16 v117, v147, v15\n"
    "v_dot2c_f32_bf16 v116, v148, v15\n"
    "v_dot2c_f32_bf16 v117, v149, v15\n"
    "v_dot2c_f32_bf16 v116, v150, v15\n"
    "v_dot2c_f32_bf16 v117, v151, v15\n"
    "v_dot2c_f32_bf16 v116, v152, v15\n"
    "v_dot2c_f32_bf16 v117, v153, v15\n"
    "v_dot2c_f32_bf16 v116, v154, v15\n"
    "v_dot2c_f32_bf16 v117, v155, v15\n"
    "v_dot2c_f32_bf16 v116, v156, v15\n"
    "v_dot2c_f32_bf16 v117, v157, v15\n"
    "v_dot2c_f32_bf16 v116, v158, v15\n"
    "v_dot2c_f32_bf16 v117, v159, v15\n"
    "s_waitcnt lgkmcnt(0)\n"
    "v_mfma_f32_32x32x16_bf16 v[96:111], v[144:147], v[80:83], v[96:111]\n"
    "v_mfma_f32_32x32x16_bf16 v[96:111], v[148:151], v[84:87], v[96:111]\n"
    "s_barrier\n"
    "ds_read_b64_tr_b16 v[128:129], v12 offset:24576\n"
    "ds_read_b64_tr_b16 v[130:131], v12 offset:24704\n"
    "ds_read_b64_tr_b16 v[132:133], v12 offset:25088\n"
    "ds_read_b64_tr_b16 v[134:135], v12 offset:25216\n"
    "ds_read_b64_tr_b16 v[136:137], v12 offset:25600\n"
    "ds_read_b64_tr_b16 v[138:139], v12 offset:25728\n"
    "ds_read_b64_tr_b16 v[140:141], v12 offset:26112\n"
    "ds_read_b64_tr_b16 v[142:143], v12 offset:26240\n"
    "ds_read_b128 v[64:67], v112 offset:32768\n"
    "ds_read_b128 v[68:71], v113 offset:32768\n"
    "ds_read_b128 v[72:75], v114 offset:32768\n"
    "ds_read_b128 v[76:79], v115 offset:32768\n"
    "v_mfma_f32_32x32x16_bf16 v[96:111], v[152:155], v[88:91], v[96:111]\n"
    "v_mfma_f32_32x32x16_bf16 v[96:111], v[156:159], v[92:95], v[96:111]\n"
    "s_waitcnt vmcnt(0)\n"
    "v_readfirstlane_b32 s56, v4\n"
    "v_readfirstlane_b32 s57, v5\n"
    "s_add_u32 s56, s56, 0x7f4a7c15\n"
    "s_addc_u32 s57, s57, 0x9e3779b9\n"
    "s_mov_b64 exec, 0xffff\n"
    "buffer_store_dwordx4 v[120:123], v118, s[28:31], s60 offen offset:1536 sc1\n"
    "s_mov_b64 exec, -1\n"
    "s_waitcnt lgkmcnt(4)\n"
    "v_dot2c_f32_bf16 v116, v128, v15\n"
    "v_dot2c_f32_bf16 v117, v129, v15\n"
    "v_dot2c_f32_bf16 v116, v130, v15\n"
    "v_dot2c_f32_bf16 v117, v131, v15\n"
    "v_dot2c_f32_bf16 v116, v132, v15\n"
    "v_dot2c_f32_bf16 v117, v133, v15\n"
    "v_dot2c_f32_bf16 v116, v134, v15\n"
    "v_dot2c_f32_bf16 v117, v135, v15\n"
    "v_dot2c_f32_bf16 v116, v136, v15\n"
    "v_dot2c_f32_bf16 v117, v137, v15\n"
    "v_dot2c_f32_bf16 v116, v138, v15\n"
    "v_dot2c_f32_bf16 v117, v139, v15\n"
    "v_dot2c_f32_bf16 v116, v140, v15\n"
    "v_dot2c_f32_bf16 v117, v141, v15\n"
    "v_dot2c_f32_bf16 v116, v142, v15\n"
    "v_dot2c_f32_bf16 v117, v143, v15\n"
    "s_waitcnt lgkmcnt(0)\n"
    "v_mfma_f32_32x32x16_bf16 v[96:111], v[128:131], v[64:67], v[96:111]\n"
    "v_mfma_f32_32x32x16_bf16 v[96:111], v[132:135], v[68:71], v[96:111]\n"
    "s_barrier\n"
    "ds_read_b64_tr_b16 v[144:145], v12 offset:28672\n"
    "ds_read_b64_tr_b16 v[146:147], v12 offset:28800\n"
    "ds_read_b64_tr_b16 v[148:149], v12 offset:29184\n"
    "ds_read_b64_tr_b16 v[150:151], v12 offset:29312\n"
    "ds_read_b64_tr_b16 v[152:153], v12 offset:29696\n"
    "ds_read_b64_tr_b16 v[154:155], v12 offset:29824\n"
    "ds_read_b64_tr_b16 v[156:157], v12 offset:30208\n"
    "ds_read_b64_tr_b16 v[158:159], v12 offset:30336\n"
    "ds_read_b128 v[80:83], v112 offset:49152\n"
    "ds_read_b128 v[84:87], v113 offset:49152\n"
    "ds_read_b128 v[88:91], v114 offset:49152\n"
    "ds_read_b128 v[92:95], v115 offset:49152\n"
    "v_mfma_f32_32x32x16_bf16 v[96:111], v[136:139], v[72:75], v[96:111]\n"
    "v_mfma_f32_32x32x16_bf16 v[96:111], v[140:143], v[76:79], v[96:111]\n"
    "s_waitcnt lgkmcnt(4)\n"
    "v_dot2c_f32_bf16 v116, v144, v15\n"
    "v_dot2c_f32_bf16 v117, v145, v15\n"
    "v_dot2c_f32_bf16 v116, v146, v15\n"
    "v_dot2c_f32_bf16 v117, v147, v15\n"
    "v_dot2c_f32_bf16 v116, v148, v15\n"
    "v_dot2c_f32_bf16 v117, v149, v15\n"
    "v_dot2c_f32_bf16 v116, v150, v15\n"
    "v_dot2c_f32_bf16 v117, v151, v15\n"
    "v_dot2c_f32_bf16 v116, v152, v15\n"
    "v_dot2c_f32_bf16 v117, v153, v15\n"
    "v_dot2c_f32_bf16 v116, v154, v15\n"
    "v_dot2c_f32_bf16 v117, v155, v15\n"
    "v_dot2c_f32_bf16 v116, v156, v15\n"
    "v_dot2c_f32_bf16 v117, v157, v15\n"
    "v_dot2c_f32_bf16 v116, v158, v15\n"
    "v_dot2c_f32_bf16 v117, v159, v15\n"
    "s_waitcnt lgkmcnt(0)\n"
    "v_mfma_f32_32x32x16_bf16 v[96:111], v[144:147], v[80:83], v[96:111]\n"
    "v_mfma_f32_32x32x16_bf16 v[96:111], v[148:151], v[84:87], v[96:111]\n"
    "v_mfma_f32_32x32x16_bf16 v[96:111], v[152:155], v[88:91], v[96:111]\n"
    "v_mfma_f32_32x32x16_bf16 v[96:111], v[156:159], v[92:95], v[96:111]\n"
    "v_add_f32 v116, v116, v117\n"
    "v_cvt_pk_bf16_f32 v40, v116, v116\n"
    "v_lshlrev_b32 v40, 16, v40\n"
    "v_sub_f32 v40, v116, v40\n"
    "v_cvt_pk_bf16_f32 v40, v116, v40\n"
    "s_barrier\n"
    "ds_read_b32 v48, v27 offset:2048\n"
    "v_subrev_u32 v112, 0x10000, v112\n"
    "v_subrev_u32 v113, 0x10000, v113\n"
    "v_subrev_u32 v114, 0x10000, v114\n"
    "v_subrev_u32 v115, 0x10000, v115\n"
    "s_waitcnt lgkmcnt(0)\n"
    "v_and_b32 v44, 0xffff, v48\n"
    "v_lshl_or_b32 v44, v44, 16, v44\n"
    "v_and_b32 v11, 0xffff0000, v48\n"
    "v_rcp_f32 v50, v11\n"
    "v_mfma_f32_32x32x16_bf16 v[96:111], v[40:43], v[44:47], v[96:111]\n"
    "v_mov_b32 v65, s56\n"
    "v_mov_b32 v67, s56\n"
    "v_mov_b32 v69, s56\n"
    "v_mov_b32 v71, s56\n"
    "v_mov_b32 v73, s56\n"
    "v_mov_b32 v75, s56\n"
    "v_mov_b32 v77, s56\n"
    "v_mov_b32 v79, s56\n"
    "s_nop 3\n"
    "v_mul_f32 v104, v50, v104\n"
    "v_mul_f32 v105, v50, v105\n"
    "v_mul_f32 v106, v50, v106\n"
    "v_mul_f32 v107, v50, v107\n"
    "v_mul_f32 v108, v50, v108\n"
    "v_mul_f32 v109, v50, v109\n"
    "v_mul_f32 v110, v50, v110\n"
    "v_mul_f32 v111, v50, v111\n"
    "v_cvt_pk_bf16_f32 v124, v104, v105\n"
    "v_cvt_pk_bf16_f32 v125, v106, v107\n"
    "v_cvt_pk_bf16_f32 v126, v108, v109\n"
    "v_cvt_pk_bf16_f32 v127, v110, v111\n"
    "ds_write_b64 v14, v[124:125]\n"
    "ds_write_b64 v14, v[126:127] offset:16\n"
    "v_mul_f32 v96, v50, v96\n"
    "v_mul_f32 v97, v50, v97\n"
    "v_mul_f32 v98, v50, v98\n"
    "v_mul_f32 v99, v50, v99\n"
    "v_mul_f32 v100, v50, v100\n"
    "v_mul_f32 v101, v50, v101\n"
    "v_mul_f32 v102, v50, v102\n"
    "v_mul_f32 v103, v50, v103\n"
    "s_waitcnt lgkmcnt(0)\n"
    "ds_read_b64_tr_b16 v[24:25], v13\n"
    "ds_read_b64_tr_b16 v[26:27], v13 offset:128\n"
    "v_cvt_pk_bf16_f32 v64, v96, v97\n"
    "v_cvt_pk_bf16_f32 v66, v98, v99\n"
    "v_cvt_pk_bf16_f32 v68, v100, v101\n"
    "v_cvt_pk_bf16_f32 v70, v102, v103\n"
    "buffer_store_dwordx4 v[64:67], v10, s[28:31], s62 offen offset:0 sc1\n"
    "buffer_store_dwordx4 v[68:71], v10, s[28:31], s62 offen offset:1024 sc1\n"
    "s_waitcnt lgkmcnt(0)\n"
    "v_mov_b32 v72, v24\n"
    "v_mov_b32 v74, v25\n"
    "v_mov_b32 v76, v26\n"
    "v_mov_b32 v78, v27\n"
    "buffer_store_dwordx4 v[72:75], v10, s[28:31], s62 offen offset:2048 sc1\n"
    "buffer_store_dwordx4 v[76:79], v10, s[28:31], s62 offen offset:3072 sc1\n"
    "v_mov_b32 v120, v64\n"
    "v_mov_b32 v121, v66\n"
    "v_mov_b32 v122, v68\n"
    "v_mov_b32 v123, v70\n"
    "buffer_store_dwordx4 v[24:27], v119, s[28:31], s60 offen offset:1024 sc1\n"
    "buffer_store_dwordx4 v[120:123], v10, s[28:31], s60 offen sc1\n"
    "s_barrier\n"
    "s_waitcnt vmcnt(0)\n"
    "v_mov_b32 v24, s56\n"
    "v_mov_b32 v25, s57\n"
    "v_lshlrev_b32 v26, 3, v1\n"
    "s_mov_b64 exec, 0xffff\n"
    "buffer_store_dwordx2 v[24:25], v26, s[28:31], s59 offen sc1\n"
    "s_mov_b64 exec, -1\n"
    "s_branch .LA_gen\n"
    ".LA_help:\n"
    "v_mov_b32 v116, 0\n"
    "v_mov_b32 v117, 0\n"
    "v_xor_b32 v112, 0x2000, v112\n"
    "v_xor_b32 v113, 0x2000, v113\n"
    "v_xor_b32 v114, 0x2000, v114\n"
    "v_xor_b32 v115, 0x2000, v115\n"
    "s_xor_b32 s59, s52, 2\n"
    "s_lshl_b32 s60, s53, 2\n"
    "s_add_u32 s59, s59, s60\n"
    "s_lshl_b32 s59, s59, 8\n"
    "s_add_u32 s59, s59, 4096\n"
    "s_lshl_b32 s61, s52, 12\n"
    "s_add_u32 s61, s61, 114688\n"
    "v_lshlrev_b32 v13, 2, v6\n"
    "v_add_u32 v13, s61, v13\n"
    "v_lshlrev_b32 v14, 3, v6\n"
    "s_barrier\n"
    "ds_read_b128 v[64:67], v112 offset:0\n"
    "ds_read_b128 v[68:71], v113 offset:0\n"
    "ds_read_b128 v[72:75], v114 offset:0\n"
    "ds_read_b128 v[76:79], v115 offset:0\n"
    "s_waitcnt lgkmcnt(0)\n"
    "v_dot2c_f32_bf16 v116, v64, v15\n"
    "v_dot2c_f32_bf16 v117, v64, v64\n"
    "v_dot2c_f32_bf16 v116, v65, v15\n"
    "v_dot2c_f32_bf16 v117, v65, v65\n"
    "v_dot2c_f32_bf16 v116, v66, v15\n"
    "v_dot2c_f32_bf16 v117, v66, v66\n"
    "v_dot2c_f32_bf16 v116, v67, v15\n"
    "v_dot2c_f32_bf16 v117, v67, v67\n"
    "v_dot2c_f32_bf16 v116, v68, v15\n"
    "v_dot2c_f32_bf16 v117, v68, v68\n"
    "v_dot2c_f32_bf16 v116, v69, v15\n"
    "v_dot2c_f32_bf16 v117, v69, v69\n"
    "v_dot2c_f32_bf16 v116, v70, v15\n"
    "v_dot2c_f32_bf16 v117, v70, v70\n"
    "v_dot2c_f32_bf16 v116, v71, v15\n"
    "v_dot2c_f32_bf16 v117, v71, v71\n"
    "s_barrier\n"
    "ds_read_b128 v[80:83], v112 offset:16384\n"
    "ds_read_b128 v[84:87], v113 offset:16384\n"
    "ds_read_b128 v[88:91], v114 offset:16384\n"
    "ds_read_b128 v[92:95], v115 offset:16384\n"
    "v_dot2c_f32_bf16 v116, v72, v15\n"
    "v_dot2c_f32_bf16 v117, v72, v72\n"
    "v_dot2c_f32_bf16 v116, v73, v15\n"
    "v_dot2c_f32_bf16 v117, v73, v73\n"
    "v_dot2c_f32_bf16 v116, v74, v15\n"
    "v_dot2c_f32_bf16 v117, v74, v74\n"
    "v_dot2c_f32_bf16 v116, v75, v15\n"
    "v_dot2c_f32_bf16 v117, v75, v75\n"
    "v_dot2c_f32_bf16 v116, v76, v15\n"
    "v_dot2c_f32_bf16 v117, v76, v76\n"
    "v_dot2c_f32_bf16 v116, v77, v15\n"
    "v_dot2c_f32_bf16 v117, v77, v77\n"
    "v_dot2c_f32_bf16 v116, v78, v15\n"
    "v_dot2c_f32_bf16 v117, v78, v78\n"
    "v_dot2c_f32_bf16 v116, v79, v15\n"
    "v_dot2c_f32_bf16 v117, v79, v79\n"
    "s_waitcnt lgkmcnt(0)\n"
    "v_dot2c_f32_bf16 v116, v80, v15\n"
    "v_dot2c_f32_bf16 v117, v80, v80\n"
    "v_dot2c_f32_bf16 v116, v81, v15\n"
    "v_dot2c_f32_bf16 v117, v81, v81\n"
    "v_dot2c_f32_bf16 v116, v82, v15\n"
    "v_dot2c_f32_bf16 v117, v82, v82\n"
    "v_dot2c_f32_bf16 v116, v83, v15\n"
    "v_dot2c_f32_bf16 v117, v83, v83\n"
    "v_dot2c_f32_bf16 v116, v84, v15\n"
    "v_dot2c_f32_bf16 v117, v84, v84\n"
    "v_dot2c_f32_bf16 v116, v85, v15\n"
    "v_dot2c_f32_bf16 v117, v85, v85\n"
    "v_dot2c_f32_bf16 v116, v86, v15\n"
    "v_dot2c_f32_bf16 v117, v86, v86\n"
    "v_dot2c_f32_bf16 v116, v87, v15\n"
    "v_dot2c_f32_bf16 v117, v87, v87\n"
    "s_barrier\n"
    "ds_read_b128 v[64:67], v112 offset:32768\n"
    "ds_read_b128 v[68:71], v113 offset:32768\n"
    "ds_read_b128 v[72:75], v114 offset:32768\n"
    "ds_read_b128 v[76:79], v115 offset:32768\n"
    "v_dot2c_f32_bf16 v116, v88, v15\n"
    "v_dot2c_f32_bf16 v117, v88, v88\n"
    "v_dot2c_f32_bf16 v116, v89, v15\n"
    "v_dot2c_f32_bf16 v117, v89, v89\n"
    "v_dot2c_f32_bf16 v116, v90, v15\n"
    "v_dot2c_f32_bf16 v117, v90, v90\n"
    "v_dot2c_f32_bf16 v116, v91, v15\n"
    "v_dot2c_f32_bf16 v117, v91, v91\n"
    "v_dot2c_f32_bf16 v116, v92, v15\n"
    "v_dot2c_f32_bf16 v117, v92, v92\n"
    "v_dot2c_f32_bf16 v116, v93, v15\n"
    "v_dot2c_f32_bf16 v117, v93, v93\n"
    "v_dot2c_f32_bf16 v116, v94, v15\n"
    "v_dot2c_f32_bf16 v117, v94, v94\n"
    "v_dot2c_f32_bf16 v116, v95, v15\n"
    "v_dot2c_f32_bf16 v117, v95, v95\n"
    "s_waitcnt lgkmcnt(0)\n"
    "v_dot2c_f32_bf16 v116, v64, v15\n"
    "v_dot2c_f32_bf16 v117, v64, v64\n"
    "v_dot2c_f32_bf16 v116, v65, v15\n"
    "v_dot2c_f32_bf16 v117, v65, v65\n"
    "v_dot2c_f32_bf16 v116, v66, v15\n"
    "v_dot2c_f32_bf16 v117, v66, v66\n"
    "v_dot2c_f32_bf16 v116, v67, v15\n"
    "v_dot2c_f32_bf16 v117, v67, v67\n"
    "v_dot2c_f32_bf16 v116, v68, v15\n"
    "v_dot2c_f32_bf16 v117, v68, v68\n"
    "v_dot2c_f32_bf16 v116, v69, v15\n"
    "v_dot2c_f32_bf16 v117, v69, v69\n"
    "v_dot2c_f32_bf16 v116, v70, v15\n"
    "v_dot2c_f32_bf16 v117, v70, v70\n"
    "v_dot2c_f32_bf16 v116, v71, v15\n"
    "v_dot2c_f32_bf16 v117, v71, v71\n"
    "s_barrier\n"
    "ds_read_b128 v[80:83], v112 offset:49152\n"
    "ds_read_b128 v[84:87], v113 offset:49152\n"
    "ds_read_b128 v[88:91], v114 offset:49152\n"
    "ds_read_b128 v[92:95], v115 offset:49152\n"
    "v_dot2c_f32_bf16 v116, v72, v15\n"
    "v_dot2c_f32_bf16 v117, v72, v72\n"
    "v_dot2c_f32_bf16 v116, v73, v15\n"
    "v_dot2c_f32_bf16 v117, v73, v73\n"
    "v_dot2c_f32_bf16 v116, v74, v15\n"
    "v_dot2c_f32_bf16 v117, v74, v74\n"
    "v_dot2c_f32_bf16 v116, v75, v15\n"
    "v_dot2c_f32_bf16 v117, v75, v75\n"
    "v_dot2c_f32_bf16 v116, v76, v15\n"
    "v_dot2c_f32_bf16 v117, v76, v76\n"
    "v_dot2c_f32_bf16 v116, v77, v15\n"
    "v_dot2c_f32_bf16 v117, v77, v77\n"
    "v_dot2c_f32_bf16 v116, v78, v15\n"
    "v_dot2c_f32_bf16 v117, v78, v78\n"
    "v_dot2c_f32_bf16 v116, v79, v15\n"
    "v_dot2c_f32_bf16 v117, v79, v79\n"
    "s_waitcnt lgkmcnt(0)\n"
    "v_dot2c_f32_bf16 v116, v80, v15\n"
    "v_dot2c_f32_bf16 v117, v80, v80\n"
    "v_dot2c_f32_bf16 v116, v81, v15\n"
    "v_dot2c_f32_bf16 v117, v81, v81\n"
    "v_dot2c_f32_bf16 v116, v82, v15\n"
    "v_dot2c_f32_bf16 v117, v82, v82\n"
    "v_dot2c_f32_bf16 v116, v83, v15\n"
    "v_dot2c_f32_bf16 v117, v83, v83\n"
    "v_dot2c_f32_bf16 v116, v84, v15\n"
    "v_dot2c_f32_bf16 v117, v84, v84\n"
    "v_dot2c_f32_bf16 v116, v85, v15\n"
    "v_dot2c_f32_bf16 v117, v85, v85\n"
    "v_dot2c_f32_bf16 v116, v86, v15\n"
    "v_dot2c_f32_bf16 v117, v86, v86\n"
    "v_dot2c_f32_bf16 v116, v87, v15\n"
    "v_dot2c_f32_bf16 v117, v87, v87\n"
    "s_barrier\n"
    "v_add_u32 v112, 0x10000, v112\n"
    "v_add_u32 v113, 0x10000, v113\n"
    "v_add_u32 v114, 0x10000, v114\n"
    "v_add_u32 v115, 0x10000, v115\n"
    "ds_read_b128 v[64:67], v112 offset:0\n"
    "ds_read_b128 v[68:71], v113 offset:0\n"
    "ds_read_b128 v[72:75], v114 offset:0\n"
    "ds_read_b128 v[76:79], v115 offset:0\n"
    "v_dot2c_f32_bf16 v116, v88, v15\n"
    "v_dot2c_f32_bf16 v117, v88, v88\n"
    "v_dot2c_f32_bf16 v116, v89, v15\n"
    "v_dot2c_f32_bf16 v117, v89, v89\n"
    "v_dot2c_f32_bf16 v116, v90, v15\n"
    "v_dot2c_f32_bf16 v117, v90, v90\n"
    "v_dot2c_f32_bf16 v116, v91, v15\n"
    "v_dot2c_f32_bf16 v117, v91, v91\n"
    "v_dot2c_f32_bf16 v116, v92, v15\n"
    "v_dot2c_f32_bf16 v117, v92, v92\n"
    "v_dot2c_f32_bf16 v116, v93, v15\n"
    "v_dot2c_f32_bf16 v117, v93, v93\n"
    "v_dot2c_f32_bf16 v116, v94, v15\n"
    "v_dot2c_f32_bf16 v117, v94, v94\n"
    "v_dot2c_f32_bf16 v116, v95, v15\n"
    "v_dot2c_f32_bf16 v117, v95, v95\n"
    "s_waitcnt lgkmcnt(0)\n"
    "v_dot2c_f32_bf16 v116, v64, v15\n"
    "v_dot2c_f32_bf16 v117, v64, v64\n"
    "v_dot2c_f32_bf16 v116, v65, v15\n"
    "v_dot2c_f32_bf16 v117, v65, v65\n"
    "v_dot2c_f32_bf16 v116, v66, v15\n"
    "v_dot2c_f32_bf16 v117, v66, v66\n"
    "v_dot2c_f32_bf16 v116, v67, v15\n"
    "v_dot2c_f32_bf16 v117, v67, v67\n"
    "v_dot2c_f32_bf16 v116, v68, v15\n"
    "v_dot2c_f32_bf16 v117, v68, v68\n"
    "v_dot2c_f32_bf16 v116, v69, v15\n"
    "v_dot2c_f32_bf16 v117, v69, v69\n"
    "v_dot2c_f32_bf16 v116, v70, v15\n"
    "v_dot2c_f32_bf16 v117, v70, v70\n"
    "v_dot2c_f32_bf16 v116, v71, v15\n"
    "v_dot2c_f32_bf16 v117, v71, v71\n"
    "s_barrier\n"
    "ds_read_b128 v[80:83], v112 offset:16384\n"
    "ds_read_b128 v[84:87], v113 offset:16384\n"
    "ds_read_b128 v[88:91], v114 offset:16384\n"
    "ds_read_b128 v[92:95], v115 offset:16384\n"
    "v_dot2c_f32_bf16 v116, v72, v15\n"
    "v_dot2c_f32_bf16 v117, v72, v72\n"
    "v_dot2c_f32_bf16 v116, v73, v15\n"
    "v_dot2c_f32_bf16 v117, v73, v73\n"
    "v_dot2c_f32_bf16 v116, v74, v15\n"
    "v_dot2c_f32_bf16 v117, v74, v74\n"
    "v_dot2c_f32_bf16 v116, v75, v15\n"
    "v_dot2c_f32_bf16 v117, v75, v75\n"
    "v_dot2c_f32_bf16 v116, v76, v15\n"
    "v_dot2c_f32_bf16 v117, v76, v76\n"
    "v_dot2c_f32_bf16 v116, v77, v15\n"
    "v_dot2c_f32_bf16 v117, v77, v77\n"
    "v_dot2c_f32_bf16 v116, v78, v15\n"
    "v_dot2c_f32_bf16 v117, v78, v78\n"
    "v_dot2c_f32_bf16 v116, v79, v15\n"
    "v_dot2c_f32_bf16 v117, v79, v79\n"
    "s_waitcnt lgkmcnt(0)\n"
    "v_dot2c_f32_bf16 v116, v80, v15\n"
    "v_dot2c_f32_bf16 v117, v80, v80\n"
    "v_dot2c_f32_bf16 v116, v81, v15\n"
    "v_dot2c_f32_bf16 v117, v81, v81\n"
    "v_dot2c_f32_bf16 v116, v82, v15\n"
    "v_dot2c_f32_bf16 v117, v82, v82\n"
    "v_dot2c_f32_bf16 v116, v83, v15\n"
    "v_dot2c_f32_bf16 v117, v83, v83\n"
    "v_dot2c_f32_bf16 v116, v84, v15\n"
    "v_dot2c_f32_bf16 v117, v84, v84\n"
    "v_dot2c_f32_bf16 v116, v85, v15\n"
    "v_dot2c_f32_bf16 v117, v85, v85\n"
    "v_dot2c_f32_bf16 v116, v86, v15\n"
    "v_dot2c_f32_bf16 v117, v86, v86\n"
    "v_dot2c_f32_bf16 v116, v87, v15\n"
    "v_dot2c_f32_bf16 v117, v87, v87\n"
    "s_barrier\n"
    "ds_read_b128 v[64:67], v112 offset:32768\n"
    "ds_read_b128 v[68:71], v113 offset:32768\n"
    "ds_read_b128 v[72:75], v114 offset:32768\n"
    "ds_read_b128 v[76:79], v115 offset:32768\n"
    "v_dot2c_f32_bf16 v116, v88, v15\n"
    "v_dot2c_f32_bf16 v117, v88, v88\n"
    "v_dot2c_f32_bf16 v116, v89, v15\n"
    "v_dot2c_f32_bf16 v117, v89, v89\n"
    "v_dot2c_f32_bf16 v116, v90, v15\n"
    "v_dot2c_f32_bf16 v117, v90, v90\n"
    "v_dot2c_f32_bf16 v116, v91, v15\n"
    "v_dot2c_f32_bf16 v117, v91, v91\n"
    "v_dot2c_f32_bf16 v116, v92, v15\n"
    "v_dot2c_f32_bf16 v117, v92, v92\n"
    "v_dot2c_f32_bf16 v116, v93, v15\n"
    "v_dot2c_f32_bf16 v117, v93, v93\n"
    "v_dot2c_f32_bf16 v116, v94, v15\n"
    "v_dot2c_f32_bf16 v117, v94, v94\n"
    "v_dot2c_f32_bf16 v116, v95, v15\n"
    "v_dot2c_f32_bf16 v117, v95, v95\n"
    "s_waitcnt vmcnt(0)\n"
    "v_readfirstlane_b32 s56, v4\n"
    "v_readfirstlane_b32 s57, v5\n"
    "s_add_u32 s56, s56, 0x7f4a7c15\n"
    "s_addc_u32 s57, s57, 0x9e3779b9\n"
    "s_waitcnt lgkmcnt(0)\n"
    "v_dot2c_f32_bf16 v116, v64, v15\n"
    "v_dot2c_f32_bf16 v117, v64, v64\n"
    "v_dot2c_f32_bf16 v116, v65, v15\n"
    "v_dot2c_f32_bf16 v117, v65, v65\n"
    "v_dot2c_f32_bf16 v116, v66, v15\n"
    "v_dot2c_f32_bf16 v117, v66, v66\n"
    "v_dot2c_f32_bf16 v116, v67, v15\n"
    "v_dot2c_f32_bf16 v117, v67, v67\n"
    "v_dot2c_f32_bf16 v116, v68, v15\n"
    "v_dot2c_f32_bf16 v117, v68, v68\n"
    "v_dot2c_f32_bf16 v116, v69, v15\n"
    "v_dot2c_f32_bf16 v117, v69, v69\n"
    "v_dot2c_f32_bf16 v116, v70, v15\n"
    "v_dot2c_f32_bf16 v117, v70, v70\n"
    "v_dot2c_f32_bf16 v116, v71, v15\n"
    "v_dot2c_f32_bf16 v117, v71, v71\n"
    "s_barrier\n"
    "ds_read_b128 v[80:83], v112 offset:49152\n"
    "ds_read_b128 v[84:87], v113 offset:49152\n"
    "ds_read_b128 v[88:91], v114 offset:49152\n"
    "ds_read_b128 v[92:95], v115 offset:49152\n"
    "v_dot2c_f32_bf16 v116, v72, v15\n"
    "v_dot2c_f32_bf16 v117, v72, v72\n"
    "v_dot2c_f32_bf16 v116, v73, v15\n"
    "v_dot2c_f32_bf16 v117, v73, v73\n"
    "v_dot2c_f32_bf16 v116, v74, v15\n"
    "v_dot2c_f32_bf16 v117, v74, v74\n"
    "v_dot2c_f32_bf16 v116, v75, v15\n"
    "v_dot2c_f32_bf16 v117, v75, v75\n"
    "v_dot2c_f32_bf16 v116, v76, v15\n"
    "v_dot2c_f32_bf16 v117, v76, v76\n"
    "v_dot2c_f32_bf16 v116, v77, v15\n"
    "v_dot2c_f32_bf16 v117, v77, v77\n"
    "v_dot2c_f32_bf16 v116, v78, v15\n"
    "v_dot2c_f32_bf16 v117, v78, v78\n"
    "v_dot2c_f32_bf16 v116, v79, v15\n"
    "v_dot2c_f32_bf16 v117, v79, v79\n"
    "s_waitcnt lgkmcnt(0)\n"
    "v_dot2c_f32_bf16 v116, v80, v15\n"
    "v_dot2c_f32_bf16 v117, v80, v80\n"
    "v_dot2c_f32_bf16 v116, v81, v15\n"
    "v_dot2c_f32_bf16 v117, v81, v81\n"
    "v_dot2c_f32_bf16 v116, v82, v15\n"
    "v_dot2c_f32_bf16 v117, v82, v82\n"
    "v_dot2c_f32_bf16 v116, v83, v15\n"
    "v_dot2c_f32_bf16 v117, v83, v83\n"
    "v_dot2c_f32_bf16 v116, v84, v15\n"
    "v_dot2c_f32_bf16 v117, v84, v84\n"
    "v_dot2c_f32_bf16 v116, v85, v15\n"
    "v_dot2c_f32_bf16 v117, v85, v85\n"
    "v_dot2c_f32_bf16 v116, v86, v15\n"
    "v_dot2c_f32_bf16 v117, v86, v86\n"
    "v_dot2c_f32_bf16 v116, v87, v15\n"
    "v_dot2c_f32_bf16 v117, v87, v87\n"
    "v_dot2c_f32_bf16 v116, v88, v15\n"
    "v_dot2c_f32_bf16 v117, v88, v88\n"
    "v_dot2c_f32_bf16 v116, v89, v15\n"
    "v_dot2c_f32_bf16 v117, v89, v89\n"
    "v_dot2c_f32_bf16 v116, v90, v15\n"
    "v_dot2c_f32_bf16 v117, v90, v90\n"
    "v_dot2c_f32_bf16 v116, v91, v15\n"
    "v_dot2c_f32_bf16 v117, v91, v91\n"
    "v_dot2c_f32_bf16 v116, v92, v15\n"
    "v_dot2c_f32_bf16 v117, v92, v92\n"
    "v_dot2c_f32_bf16 v116, v93, v15\n"
    "v_dot2c_f32_bf16 v117, v93, v93\n"
    "v_dot2c_f32_bf16 v116, v94, v15\n"
    "v_dot2c_f32_bf16 v117, v94, v94\n"
    "v_dot2c_f32_bf16 v116, v95, v15\n"
    "v_dot2c_f32_bf16 v117, v95, v95\n"
    "s_nop 2\n"
    "v_mov_b32 v24, v116\n"
    "v_mov_b32 v25, v117\n"
    "s_nop 1\n"
    "v_permlane32_swap_b32 v116, v24\n"
    "v_permlane32_swap_b32 v117, v25\n"
    "v_add_f32 v24, v24, v116\n"
    "v_add_f32 v25, v25, v117\n"
    "v_mul_f32 v24, 0x3b000000, v24\n"
    "v_mul_f32 v25, 0x3b000000, v25\n"
    "v_fma_f32 v25, -v24, v24, v25\n"
    "v_max_f32 v25, 0, v25\n"
    "v_cvt_pk_bf16_f32 v25, v25, v25\n"
    "v_lshlrev_b32 v25, 16, v25\n"
    "v_add_f32 v25, 0x3727c5ac, v25\n"
    "v_cvt_pk_bf16_f32 v25, v25, v25\n"
    "v_lshlrev_b32 v25, 16, v25\n"
    "v_sqrt_f32 v25, v25\n"
    "v_mov_b32 v27, s56\n"
    "v_cvt_pk_bf16_f32 v26, -v24, v25\n"
    "ds_write_b32 v13, v26 offset:2048\n"
    "s_mov_b32 exec_hi, 0\n"
    "buffer_store_dwordx2 v[26:27], v14, s[28:31], s59 offen sc1\n"
    "s_mov_b32 exec_hi, -1\n"
    "s_waitcnt lgkmcnt(0)\n"
    "s_barrier\n"
    "v_xor_b32 v112, 0x12000, v112\n"
    "v_xor_b32 v113, 0x12000, v113\n"
    "v_xor_b32 v114, 0x12000, v114\n"
    "v_xor_b32 v115, 0x12000, v115\n"
    "s_barrier\n"
    ".LA_gen:\n"
    "v_lshl_add_u32 v12, v7, 3, v8\n"
    "v_lshl_add_u32 v12, v12, 6, v9\n"
    "v_add_u32 v12, 131072, v12\n"
    "s_lshl_b32 s60, s53, 2\n"
    "s_add_u32 s60, s60, s52\n"
    "s_lshl_b32 s60, s60, 8\n"
    "s_add_u32 s60, s60, 4096\n"
    "v_lshlrev_b32 v118, 3, v6\n"
    "s_lshl_b32 s62, s52, 11\n"
    "s_add_u32 s62, s62, 20480\n"
    "s_mul_i32 s93, s52, 36864\n"
    "s_add_u32 s93, s93, 65536\n"
    "s_mov_b32 s94, 0\n"
    "s_lshl_b32 s86, s52, 4\n"
    "s_add_u32 s86, s86, s53\n"
    "s_lshl_b32 s86, s86, 12\n"
    "s_add_u32 s86, s86, 262144\n"
    "s_lshr_b32 s59, s53, 2\n"
    "s_lshl_b32 s59, s59, 4\n"
    "s_bfm_b64 s[70:71], 16, s59\n"
    "s_add_u32 s94, s94, 1\n"
    "s_xor_b32 s92, s94, s53\n"
    "s_mul_i32 s92, s92, 2304\n"
    "s_add_u32 s92, s92, s93\n"
    "s_lshl_b32 s90, s53, 2\n"
    "s_add_u32 s90, s90, s52\n"
    "s_lshl_b32 s90, s90, 4\n"
    "s_add_u32 s90, s90, s54\n"
    "s_lshl_b32 s91, s90, 7\n"
    "s_add_u32 s91, s91, 655360\n"
    "s_lshl_b32 s90, s90, 11\n"
    "s_add_u32 s90, s90, 1048576\n"
    "v_lshlrev_b32 v61, 2, v1\n"
    "s_mul_i32 s59, s52, 13824\n"
    "s_add_u32 s59, s59, 0\n"
    "v_add_u32 v26, s59, v10\n"
    "v_add_u32 v27, s59, v61\n"
    "s_sub_u32 s59, s52, 1\n"
    "s_and_b32 s59, s59, 3\n"
    "s_mul_i32 s59, s59, 13824\n"
    "s_add_u32 s59, s59, 0\n"
    "v_add_u32 v20, s59, v10\n"
    "v_add_u32 v21, s59, v61\n"
    "s_sub_u32 s59, s52, 2\n"
    "s_and_b32 s59, s59, 3\n"
    "s_mul_i32 s59, s59, 13824\n"
    "s_add_u32 s59, s59, 0\n"
    "v_add_u32 v22, s59, v10\n"
    "v_add_u32 v23, s59, v61\n"
    "s_sub_u32 s59, s52, 3\n"
    "s_and_b32 s59, s59, 3\n"
    "s_mul_i32 s59, s59, 13824\n"
    "s_add_u32 s59, s59, 0\n"
    "v_add_u32 v24, s59, v10\n"
    "v_add_u32 v25, s59, v61\n"
    "s_add_u32 s59, s52, 1\n"
    "s_and_b32 s59, s59, 3\n"
    "s_mul_i32 s59, s59, 6144\n"
    "s_add_u32 s59, s59, 135168\n"
    "v_add_u32 v56, s59, v10\n"
    "s_add_u32 s59, s52, 2\n"
    "s_and_b32 s59, s59, 3\n"
    "s_mul_i32 s59, s59, 6144\n"
    "s_add_u32 s59, s59, 135168\n"
    "v_add_u32 v57, s59, v10\n"
    "s_add_u32 s59, s52, 3\n"
    "s_and_b32 s59, s59, 3\n"
    "s_mul_i32 s59, s59, 6144\n"
    "s_add_u32 s59, s59, 135168\n"
    "v_add_u32 v58, s59, v10\n"
    "s_add_u32 s59, s52, 4\n"
    "s_and_b32 s59, s59, 3\n"
    "s_mul_i32 s59, s59, 6144\n"
    "s_add_u32 s59, s59, 135168\n"
    "v_add_u32 v59, s59, v10\n"
    "s_lshl_b32 s59, s52, 7\n"
    "s_add_u32 s59, s59, 155648\n"
    "v_lshlrev_b32 v60, 2, v6\n"
    "v_lshl_add_u32 v60, v7, 9, v60\n"
    "v_add_u32 v60, s59, v60\n"
    "v_bfe_u32 v13, v1, 2, 4\n"
    "v_lshlrev_b32 v13, 7, v13\n"
    "v_mov_b32 v14, 156672\n"
    "v_mov_b32 v16, -1\n"
    "v_mov_b32 v17, -1\n"
    "v_mov_b32 v18, -1\n"
    "v_mov_b32 v19, -1\n"
    "v_mov_b32 v116, 0\n"
    "v_mov_b32 v117, 0\n"
    "s_barrier\n"
    "ds_read_b64_tr_b16 v[128:129], v12 offset:0\n"
    "ds_read_b64_tr_b16 v[130:131], v12 offset:256\n"
    "ds_read_b64_tr_b16 v[132:133], v12 offset:1024\n"
    "ds_read_b64_tr_b16 v[134:135], v12 offset:1280\n"
    "ds_read_b64_tr_b16 v[136:137], v12 offset:2048\n"
    "ds_read_b64_tr_b16 v[138:139], v12 offset:2304\n"
    "ds_read_b64_tr_b16 v[140:141], v12 offset:3072\n"
    "ds_read_b64_tr_b16 v[142:143], v12 offset:3328\n"
    "ds_read_b128 v[64:67], v112 offset:0\n"
    "ds_read_b128 v[68:71], v113 offset:0\n"
    "ds_read_b128 v[72:75], v114 offset:0\n"
    "ds_read_b128 v[76:79], v115 offset:0\n"
    "s_waitcnt lgkmcnt(4)\n"
    "v_dot2c_f32_bf16 v116, v128, v15\n"
    "v_dot2c_f32_bf16 v117, v129, v15\n"
    "v_dot2c_f32_bf16 v116, v130, v15\n"
    "v_dot2c_f32_bf16 v117, v131, v15\n"
    "v_dot2c_f32_bf16 v116, v132, v15\n"
    "v_dot2c_f32_bf16 v117, v133, v15\n"
    "v_dot2c_f32_bf16 v116, v134, v15\n"
    "v_dot2c_f32_bf16 v117, v135, v15\n"
    "v_dot2c_f32_bf16 v116, v136, v15\n"
    "v_dot2c_f32_bf16 v117, v137, v15\n"
    "v_dot2c_f32_bf16 v116, v138, v15\n"
    "v_dot2c_f32_bf16 v117, v139, v15\n"
    "v_dot2c_f32_bf16 v116, v140, v15\n"
    "v_dot2c_f32_bf16 v117, v141, v15\n"
    "v_dot2c_f32_bf16 v116, v142, v15\n"
    "v_dot2c_f32_bf16 v117, v143, v15\n"
    "s_waitcnt lgkmcnt(0)\n"
    "v_mfma_f32_32x32x16_bf16 v[96:111], v[128:131], v[64:67], 0\n"
    "v_mfma_f32_32x32x16_bf16 v[96:111], v[132:135], v[68:71], v[96:111]\n"
    "s_barrier\n"
    "ds_read_b64_tr_b16 v[144:145], v12 offset:6144\n"
    "ds_read_b64_tr_b16 v[146:147], v12 offset:6400\n"
    "ds_read_b64_tr_b16 v[148:149], v12 offset:7168\n"
    "ds_read_b64_tr_b16 v[150:151], v12 offset:7424\n"
    "ds_read_b64_tr_b16 v[152:153], v12 offset:8192\n"
    "ds_read_b64_tr_b16 v[154:155], v12 offset:8448\n"
    "ds_read_b64_tr_b16 v[156:157], v12 offset:9216\n"
    "ds_read_b64_tr_b16 v[158:159], v12 offset:9472\n"
    "ds_read_b128 v[80:83], v112 offset:16384\n"
    "ds_read_b128 v[84:87], v113 offset:16384\n"
    "ds_read_b128 v[88:91], v114 offset:16384\n"
    "ds_read_b128 v[92:95], v115 offset:16384\n"
    "v_mfma_f32_32x32x16_bf16 v[96:111], v[136:139], v[72:75], v[96:111]\n"
    "v_mfma_f32_32x32x16_bf16 v[96:111], v[140:143], v[76:79], v[96:111]\n"
    "s_waitcnt lgkmcnt(4)\n"
    "v_dot2c_f32_bf16 v116, v144, v15\n"
    "v_dot2c_f32_bf16 v117, v145, v15\n"
    "v_dot2c_f32_bf16 v116, v146, v15\n"
    "v_dot2c_f32_bf16 v117, v147, v15\n"
    "v_dot2c_f32_bf16 v116, v148, v15\n"
    "v_dot2c_f32_bf16 v117, v149, v15\n"
    "v_dot2c_f32_bf16 v116, v150, v15\n"
    "v_dot2c_f32_bf16 v117, v151, v15\n"
    "v_dot2c_f32_bf16 v116, v152, v15\n"
    "v_dot2c_f32_bf16 v117, v153, v15\n"
    "v_dot2c_f32_bf16 v116, v154, v15\n"
    "v_dot2c_f32_bf16 v117, v155, v15\n"
    "v_dot2c_f32_bf16 v116, v156, v15\n"
    "v_dot2c_f32_bf16 v117, v157, v15\n"
    "v_dot2c_f32_bf16 v116, v158, v15\n"
    "v_dot2c_f32_bf16 v117, v159, v15\n"
    "s_waitcnt lgkmcnt(0)\n"
    "v_mfma_f32_32x32x16_bf16 v[96:111], v[144:147], v[80:83], v[96:111]\n"
    "v_mfma_f32_32x32x16_bf16 v[96:111], v[148:151], v[84:87], v[96:111]\n"
    "s_barrier\n"
    "ds_read_b64_tr_b16 v[128:129], v12 offset:12288\n"
    "ds_read_b64_tr_b16 v[130:131], v12 offset:12544\n"
    "ds_read_b64_tr_b16 v[132:133], v12 offset:13312\n"
    "ds_read_b64_tr_b16 v[134:135], v12 offset:13568\n"
    "ds_read_b64_tr_b16 v[136:137], v12 offset:14336\n"
    "ds_read_b64_tr_b16 v[138:139], v12 offset:14592\n"
    "ds_read_b64_tr_b16 v[140:141], v12 offset:15360\n"
    "ds_read_b64_tr_b16 v[142:143], v12 offset:15616\n"
    "ds_read_b128 v[64:67], v112 offset:32768\n"
    "ds_read_b128 v[68:71], v113 offset:32768\n"
    "ds_read_b128 v[72:75], v114 offset:32768\n"
    "ds_read_b128 v[76:79], v115 offset:32768\n"
    "v_mfma_f32_32x32x16_bf16 v[96:111], v[152:155], v[88:91], v[96:111]\n"
    "v_mfma_f32_32x32x16_bf16 v[96:111], v[156:159], v[92:95], v[96:111]\n"
    "s_waitcnt lgkmcnt(4)\n"
    "v_dot2c_f32_bf16 v116, v128, v15\n"
    "v_dot2c_f32_bf16 v117, v129, v15\n"
    "v_dot2c_f32_bf16 v116, v130, v15\n"
    "v_dot2c_f32_bf16 v117, v131, v15\n"
    "v_dot2c_f32_bf16 v116, v132, v15\n"
    "v_dot2c_f32_bf16 v117, v133, v15\n"
    "v_dot2c_f32_bf16 v116, v134, v15\n"
    "v_dot2c_f32_bf16 v117, v135, v15\n"
    "v_dot2c_f32_bf16 v116, v136, v15\n"
    "v_dot2c_f32_bf16 v117, v137, v15\n"
    "v_dot2c_f32_bf16 v116, v138, v15\n"
    "v_dot2c_f32_bf16 v117, v139, v15\n"
    "v_dot2c_f32_bf16 v116, v140, v15\n"
    "v_dot2c_f32_bf16 v117, v141, v15\n"
    "v_dot2c_f32_bf16 v116, v142, v15\n"
    "v_dot2c_f32_bf16 v117, v143, v15\n"
    "s_waitcnt lgkmcnt(0)\n"
    "v_mfma_f32_32x32x16_bf16 v[96:111], v[128:131], v[64:67], v[96:111]\n"
    "v_mfma_f32_32x32x16_bf16 v[96:111], v[132:135], v[68:71], v[96:111]\n"
    "s_barrier\n"
    "ds_read_b64_tr_b16 v[144:145], v12 offset:18432\n"
    "ds_read_b64_tr_b16 v[146:147], v12 offset:18688\n"
    "ds_read_b64_tr_b16 v[148:149], v12 offset:19456\n"
    "ds_read_b64_tr_b16 v[150:151], v12 offset:19712\n"
    "ds_read_b64_tr_b16 v[152:153], v12 offset:20480\n"
    "ds_read_b64_tr_b16 v[154:155], v12 offset:20736\n"
    "ds_read_b64_tr_b16 v[156:157], v12 offset:21504\n"
    "ds_read_b64_tr_b16 v[158:159], v12 offset:21760\n"
    "ds_read_b128 v[80:83], v112 offset:49152\n"
    "ds_read_b128 v[84:87], v113 offset:49152\n"
    "ds_read_b128 v[88:91], v114 offset:49152\n"
    "ds_read_b128 v[92:95], v115 offset:49152\n"
    "v_mfma_f32_32x32x16_bf16 v[96:111], v[136:139], v[72:75], v[96:111]\n"
    "v_mfma_f32_32x32x16_bf16 v[96:111], v[140:143], v[76:79], v[96:111]\n"
    "s_waitcnt lgkmcnt(4)\n"
    "v_dot2c_f32_bf16 v116, v144, v15\n"
    "v_dot2c_f32_bf16 v117, v145, v15\n"
    "v_dot2c_f32_bf16 v116, v146, v15\n"
    "v_dot2c_f32_bf16 v117, v147, v15\n"
    "v_dot2c_f32_bf16 v116, v148, v15\n"
    "v_dot2c_f32_bf16 v117, v149, v15\n"
    "v_dot2c_f32_bf16 v116, v150, v15\n"
    "v_dot2c_f32_bf16 v117, v151, v15\n"
    "v_dot2c_f32_bf16 v116, v152, v15\n"
    "v_dot2c_f32_bf16 v117, v153, v15\n"
    "v_dot2c_f32_bf16 v116, v154, v15\n"
    "v_dot2c_f32_bf16 v117, v155, v15\n"
    "v_dot2c_f32_bf16 v116, v156, v15\n"
    "v_dot2c_f32_bf16 v117, v157, v15\n"
    "v_dot2c_f32_bf16 v116, v158, v15\n"
    "v_dot2c_f32_bf16 v117, v159, v15\n"
    "s_waitcnt lgkmcnt(0)\n"
    "v_mfma_f32_32x32x16_bf16 v[96:111], v[144:147], v[80:83], v[96:111]\n"
    "v_mfma_f32_32x32x16_bf16 v[96:111], v[148:151], v[84:87], v[96:111]\n"
    "s_barrier\n"
    "v_add_u32 v112, 0x10000, v112\n"
    "v_add_u32 v113, 0x10000, v113\n"
    "v_add_u32 v114, 0x10000, v114\n"
    "v_add_u32 v115, 0x10000, v115\n"
    "ds_read_b64_tr_b16 v[128:129], v12 offset:4096\n"
    "ds_read_b64_tr_b16 v[130:131], v12 offset:4352\n"
    "ds_read_b64_tr_b16 v[132:133], v12 offset:5120\n"
    "ds_read_b64_tr_b16 v[134:135], v12 offset:5376\n"
    "ds_read_b64_tr_b16 v[136:137], v12 offset:10240\n"
    "ds_read_b64_tr_b16 v[138:139], v12 offset:10496\n"
    "ds_read_b64_tr_b16 v[140:141], v12 offset:11264\n"
    "ds_read_b64_tr_b16 v[142:143], v12 offset:11520\n"
    "ds_read_b128 v[64:67], v112 offset:0\n"
    "ds_read_b128 v[68:71], v113 offset:0\n"
    "ds_read_b128 v[72:75], v114 offset:0\n"
    "ds_read_b128 v[76:79], v115 offset:0\n"
    "v_mfma_f32_32x32x16_bf16 v[96:111], v[152:155], v[88:91], v[96:111]\n"
    "v_mfma_f32_32x32x16_bf16 v[96:111], v[156:159], v[92:95], v[96:111]\n"
    "s_waitcnt lgkmcnt(4)\n"
    "v_dot2c_f32_bf16 v116, v128, v15\n"
    "v_dot2c_f32_bf16 v117, v129, v15\n"
    "v_dot2c_f32_bf16 v116, v130, v15\n"
    "v_dot2c_f32_bf16 v117, v131, v15\n"
    "v_dot2c_f32_bf16 v116, v132, v15\n"
    "v_dot2c_f32_bf16 v117, v133, v15\n"
    "v_dot2c_f32_bf16 v116, v134, v15\n"
    "v_dot2c_f32_bf16 v117, v135, v15\n"
    "v_dot2c_f32_bf16 v116, v136, v15\n"
    "v_dot2c_f32_bf16 v117, v137, v15\n"
    "v_dot2c_f32_bf16 v116, v138, v15\n"
    "v_dot2c_f32_bf16 v117, v139, v15\n"
    "v_dot2c_f32_bf16 v116, v140, v15\n"
    "v_dot2c_f32_bf16 v117, v141, v15\n"
    "v_dot2c_f32_bf16 v116, v142, v15\n"
    "v_dot2c_f32_bf16 v117, v143, v15\n"
    "s_waitcnt lgkmcnt(0)\n"
    "v_mfma_f32_32x32x16_bf16 v[96:111], v[128:131], v[64:67], v[96:111]\n"
    "v_mfma_f32_32x32x16_bf16 v[96:111], v[132:135], v[68:71], v[96:111]\n"
    "s_barrier\n"
    "ds_read_b64_tr_b16 v[144:145], v12 offset:16384\n"
    "ds_read_b64_tr_b16 v[146:147], v12 offset:16640\n"
    "ds_read_b64_tr_b16 v[148:149], v12 offset:17408\n"
    "ds_read_b64_tr_b16 v[150:151], v12 offset:17664\n"
    "ds_read_b64_tr_b16 v[152:153], v12 offset:22528\n"
    "ds_read_b64_tr_b16 v[154:155], v12 offset:22784\n"
    "ds_read_b64_tr_b16 v[156:157], v12 offset:23552\n"
    "ds_read_b64_tr_b16 v[158:159], v12 offset:23808\n"
    "ds_read_b128 v[80:83], v112 offset:16384\n"
    "ds_read_b128 v[84:87], v113 offset:16384\n"
    "ds_read_b128 v[88:91], v114 offset:16384\n"
    "ds_read_b128 v[92:95], v115 offset:16384\n"
    "v_mfma_f32_32x32x16_bf16 v[96:111], v[136:139], v[72:75], v[96:111]\n"
    "v_mfma_f32_32x32x16_bf16 v[96:111], v[140:143], v[76:79], v[96:111]\n"
    "s_waitcnt lgkmcnt(4)\n"
    "v_dot2c_f32_bf16 v116, v144, v15\n"
    "v_dot2c_f32_bf16 v117, v145, v15\n"
    "v_dot2c_f32_bf16 v116, v146, v15\n"
    "v_dot2c_f32_bf16 v117, v147, v15\n"
    "v_dot2c_f32_bf16 v116, v148, v15\n"
    "v_dot2c_f32_bf16 v117, v149, v15\n"
    "v_dot2c_f32_bf16 v116, v150, v15\n"
    "v_dot2c_f32_bf16 v117, v151, v15\n"
    "v_dot2c_f32_bf16 v116, v152, v15\n"
    "v_dot2c_f32_bf16 v117, v153, v15\n"
    "v_dot2c_f32_bf16 v116, v154, v15\n"
    "v_dot2c_f32_bf16 v117, v155, v15\n"
    "v_dot2c_f32_bf16 v116, v156, v15\n"
    "v_dot2c_f32_bf16 v117, v157, v15\n"
    "v_dot2c_f32_bf16 v116, v158, v15\n"
    "v_dot2c_f32_bf16 v117, v159, v15\n"
    "s_waitcnt lgkmcnt(0)\n"
    "v_mfma_f32_32x32x16_bf16 v[96:111], v[144:147], v[80:83], v[96:111]\n"
    "v_mfma_f32_32x32x16_bf16 v[96:111], v[148:151], v[84:87], v[96:111]\n"
    "s_barrier\n"
    "ds_read_b64_tr_b16 v[128:129], v12 offset:24576\n"
    "ds_read_b64_tr_b16 v[130:131], v12 offset:24832\n"
    "ds_read_b64_tr_b16 v[132:133], v12 offset:25600\n"
    "ds_read_b64_tr_b16 v[134:135], v12 offset:25856\n"
    "ds_read_b64_tr_b16 v[136:137], v12 offset:26624\n"
    "ds_read_b64_tr_b16 v[138:139], v12 offset:26880\n"
    "ds_read_b64_tr_b16 v[140:141], v12 offset:27648\n"
    "ds_read_b64_tr_b16 v[142:143], v12 offset:27904\n"
    "ds_read_b128 v[64:67], v112 offset:32768\n"
    "ds_read_b128 v[68:71], v113 offset:32768\n"
    "ds_read_b128 v[72:75], v114 offset:32768\n"
    "ds_read_b128 v[76:79], v115 offset:32768\n"
    "v_mfma_f32_32x32x16_bf16 v[96:111], v[152:155], v[88:91], v[96:111]\n"
    "v_mfma_f32_32x32x16_bf16 v[96:111], v[156:159], v[92:95], v[96:111]\n"
    "s_waitcnt vmcnt(0)\n"
    "v_readfirstlane_b32 s56, v4\n"
    "v_readfirstlane_b32 s57, v5\n"
    "s_add_u32 s56, s56, 0x7f4a7c15\n"
    "s_addc_u32 s57, s57, 0x9e3779b9\n"
    "s_waitcnt lgkmcnt(4)\n"
    "v_dot2c_f32_bf16 v116, v128, v15\n"
    "v_dot2c_f32_bf16 v117, v129, v15\n"
    "v_dot2c_f32_bf16 v116, v130, v15\n"
    "v_dot2c_f32_bf16 v117, v131, v15\n"
    "v_dot2c_f32_bf16 v116, v132, v15\n"
    "v_dot2c_f32_bf16 v117, v133, v15\n"
    "v_dot2c_f32_bf16 v116, v134, v15\n"
    "v_dot2c_f32_bf16 v117, v135, v15\n"
    "v_dot2c_f32_bf16 v116, v136, v15\n"
    "v_dot2c_f32_bf16 v117, v137, v15\n"
    "v_dot2c_f32_bf16 v116, v138, v15\n"
    "v_dot2c_f32_bf16 v117, v139, v15\n"
    "v_dot2c_f32_bf16 v116, v140, v15\n"
    "v_dot2c_f32_bf16 v117, v141, v15\n"
    "v_dot2c_f32_bf16 v116, v142, v15\n"
    "v_dot2c_f32_bf16 v117, v143, v15\n"
    "s_waitcnt lgkmcnt(0)\n"
    "v_mfma_f32_32x32x16_bf16 v[96:111], v[128:131], v[64:67], v[96:111]\n"
    "v_mfma_f32_32x32x16_bf16 v[96:111], v[132:135], v[68:71], v[96:111]\n"
    "s_barrier\n"
    "ds_read_b64_tr_b16 v[144:145], v12 offset:28672\n"
    "ds_read_b64_tr_b16 v[146:147], v12 offset:28928\n"
    "ds_read_b64_tr_b16 v[148:149], v12 offset:29696\n"
    "ds_read_b64_tr_b16 v[150:151], v12 offset:29952\n"
    "ds_read_b64_tr_b16 v[152:153], v12 offset:30720\n"
    "ds_read_b64_tr_b16 v[154:155], v12 offset:30976\n"
    "ds_read_b64_tr_b16 v[156:157], v12 offset:31744\n"
    "ds_read_b64_tr_b16 v[158:159], v12 offset:32000\n"
    "ds_read_b128 v[80:83], v112 offset:49152\n"
    "ds_read_b128 v[84:87], v113 offset:49152\n"
    "ds_read_b128 v[88:91], v114 offset:49152\n"
    "ds_read_b128 v[92:95], v115 offset:49152\n"
    "v_mfma_f32_32x32x16_bf16 v[96:111], v[136:139], v[72:75], v[96:111]\n"
    "v_mfma_f32_32x32x16_bf16 v[96:111], v[140:143], v[76:79], v[96:111]\n"
    "buffer_load_dwordx2 v[48:49], v118, s[28:31], s60 offen sc1\n"
    "s_waitcnt lgkmcnt(4)\n"
    "v_dot2c_f32_bf16 v116, v144, v15\n"
    "v_dot2c_f32_bf16 v117, v145, v15\n"
    "v_dot2c_f32_bf16 v116, v146, v15\n"
    "v_dot2c_f32_bf16 v117, v147, v15\n"
    "v_dot2c_f32_bf16 v116, v148, v15\n"
    "v_dot2c_f32_bf16 v117, v149, v15\n"
    "v_dot2c_f32_bf16 v116, v150, v15\n"
    "v_dot2c_f32_bf16 v117, v151, v15\n"
    "v_dot2c_f32_bf16 v116, v152, v15\n"
    "v_dot2c_f32_bf16 v117, v153, v15\n"
    "v_dot2c_f32_bf16 v116, v154, v15\n"
    "v_dot2c_f32_bf16 v117, v155, v15\n"
    "v_dot2c_f32_bf16 v116, v156, v15\n"
    "v_dot2c_f32_bf16 v117, v157, v15\n"
    "v_dot2c_f32_bf16 v116, v158, v15\n"
    "v_dot2c_f32_bf16 v117, v159, v15\n"
    "s_waitcnt lgkmcnt(0)\n"
    "v_mfma_f32_32x32x16_bf16 v[96:111], v[144:147], v[80:83], v[96:111]\n"
    "v_mfma_f32_32x32x16_bf16 v[96:111], v[148:151], v[84:87], v[96:111]\n"
    "v_mfma_f32_32x32x16_bf16 v[96:111], v[152:155], v[88:91], v[96:111]\n"
    "v_mfma_f32_32x32x16_bf16 v[96:111], v[156:159], v[92:95], v[96:111]\n"
    "v_add_f32 v116, v116, v117\n"
    "v_cvt_pk_bf16_f32 v40, v116, v116\n"
    "v_lshlrev_b32 v40, 16, v40\n"
    "v_sub_f32 v40, v116, v40\n"
    "v_cvt_pk_bf16_f32 v40, v116, v40\n"
    "v_and_b32 v4, 27, v6\n"
    "v_cmp_eq_u32 vcc, 16, v4\n"
    "v_and_b32 v4, 15, v6\n"
    "v_lshlrev_b32 v4, 4, v4\n"
    "v_cndmask_b32 v5, 0, v15, vcc\n"
    "v_lshl_add_u32 v4, v7, 8, v4\n"
    "v_mov_b32 v15, 1\n"
    "v_cmp_eq_u32 s[68:69], 0, v7\n"
    "v_cmp_gt_u32 s[72:73], 16, v6\n"
    "s_mov_b32 s67, 100000\n"
    ".LA_st:\n"
    "s_waitcnt vmcnt(0)\n"
    "v_cmp_ne_u32 vcc, s56, v49\n"
    "s_nop 1\n"
    "s_cmp_eq_u64 vcc, 0\n"
    "s_cbranch_scc1 .LA_std\n"
    "buffer_load_dwordx2 v[48:49], v118, s[28:31], s60 offen sc1\n"
    "s_sub_u32 s67, s67, 1\n"
    "s_cbranch_scc0 .LA_st\n"
    "s_waitcnt vmcnt(0)\n"
    ".LA_std:\n"
    "v_and_b32 v44, 0xffff, v48\n"
    "v_lshl_or_b32 v44, v44, 16, v44\n"
    "v_and_b32 v11, 0xffff0000, v48\n"
    "v_rcp_f32 v50, v11\n"
    "v_mfma_f32_32x32x16_bf16 v[96:111], v[40:43], v[44:47], v[96:111]\n"
    "v_mul_f32 v51, 0x3eb8aa3b, v50\n"
    "v_mul_f32 v11, v11, v11\n"
    "v_cndmask_b32 v61, v50, v44, s[68:69]\n"
    "ds_write_b32 v60, v61\n"
    "v_mfma_f32_32x32x16_bf16 v[128:143], v[52:55], v[52:55], 0\n"
    "v_mfma_f32_32x32x16_bf16 v[144:159], v[52:55], v[52:55], 0\n"
    "s_nop 5\n"
    "v_mul_f32 v96, v51, v96\n"
    "v_mul_f32 v97, v51, v97\n"
    "v_mul_f32 v98, v51, v98\n"
    "v_mul_f32 v99, v51, v99\n"
    "v_mul_f32 v100, v51, v100\n"
    "v_mul_f32 v101, v51, v101\n"
    "v_mul_f32 v102, v51, v102\n"
    "v_mul_f32 v103, v51, v103\n"
    "v_cvt_pk_bf16_f32 v120, v96, v97\n"
    "v_cvt_pk_bf16_f32 v121, v98, v99\n"
    "v_cvt_pk_bf16_f32 v122, v100, v101\n"
    "v_cvt_pk_bf16_f32 v123, v102, v103\n"
    "ds_write_b128 v59, v[120:123] offset:0\n"
    "v_mul_f32 v104, v51, v104\n"
    "v_mul_f32 v105, v51, v105\n"
    "v_mul_f32 v106, v51, v106\n"
    "v_mul_f32 v107, v51, v107\n"
    "v_mul_f32 v108, v51, v108\n"
    "v_mul_f32 v109, v51, v109\n"
    "v_mul_f32 v110, v51, v110\n"
    "v_mul_f32 v111, v51, v111\n"
    "v_cvt_pk_bf16_f32 v124, v104, v105\n"
    "v_cvt_pk_bf16_f32 v125, v106, v107\n"
    "v_cvt_pk_bf16_f32 v126, v108, v109\n"
    "v_cvt_pk_bf16_f32 v127, v110, v111\n"
    "ds_write_b128 v59, v[124:127] offset:1024\n"
    "buffer_load_dwordx4 v[64:67], v10, s[28:31], s86 offen offset:0 sc1\n"
    "buffer_load_dwordx4 v[68:71], v10, s[28:31], s86 offen offset:1024 sc1\n"
    "buffer_load_dwordx4 v[72:75], v4, s[28:31], s86 offen offset:2048 sc1\n"
    "buffer_load_dwordx4 v[76:79], v4, s[28:31], s86 offen offset:3072 sc1\n"
    "buffer_load_dwordx4 v[80:83], v4, s[28:31], s86 offen offset:2560 sc1\n"
    "buffer_load_dwordx4 v[84:87], v4, s[28:31], s86 offen offset:3584 sc1\n"
    "s_waitcnt lgkmcnt(0)\n"
    "s_barrier\n"
    "ds_read_b128 v[96:99], v56 offset:0\n"
    "ds_read_b128 v[100:103], v56 offset:1024\n"
    "ds_read_b128 v[104:107], v57 offset:0\n"
    "ds_read_b128 v[108:111], v57 offset:1024\n"
    "ds_read_b128 v[112:115], v58 offset:0\n"
    "ds_read_b128 v[116:119], v58 offset:1024\n"
    "buffer_load_dwordx2 v[62:63], v13, s[28:31], s62 offen sc1\n"
    "s_mov_b32 s67, 100000\n"
    "s_waitcnt lgkmcnt(0)\n"
    "s_barrier\n"
    "s_setprio 2\n"
    "s_waitcnt vmcnt(1)\n"
    ".LA_grchk:\n"
    "v_xor_b32 v88, s56, v65\n"
    "v_xor_b32 v89, s56, v67\n"
    "v_bitop3_b32 v88, v88, v69, s56 bitop3:0xf6\n"
    "v_bitop3_b32 v89, v89, v71, s56 bitop3:0xf6\n"
    "v_bitop3_b32 v88, v88, v73, s56 bitop3:0xf6\n"
    "v_bitop3_b32 v89, v89, v75, s56 bitop3:0xf6\n"
    "v_bitop3_b32 v88, v88, v77, s56 bitop3:0xf6\n"
    "v_bitop3_b32 v89, v89, v79, s56 bitop3:0xf6\n"
    "v_bitop3_b32 v88, v88, v81, s56 bitop3:0xf6\n"
    "v_bitop3_b32 v89, v89, v83, s56 bitop3:0xf6\n"
    "v_bitop3_b32 v88, v88, v85, s56 bitop3:0xf6\n"
    "v_bitop3_b32 v89, v89, v87, s56 bitop3:0xf6\n"
    "v_or_b32 v88, v88, v89\n"
    "v_cmp_ne_u32 vcc, 0, v88\n"
    "v_mov_b32 v28, v64\n"
    "v_mov_b32 v29, v66\n"
    "v_mov_b32 v30, v68\n"
    "v_mov_b32 v31, v70\n"
    "s_cmp_lg_u64 vcc, 0\n"
    "s_cbranch_scc1 .LA_grwait\n"
    ".LA_grok:\n"
    "v_cndmask_b32 v32, v5, v72, s[72:73]\n"
    "v_cndmask_b32 v33, v5, v74, s[72:73]\n"
    "v_cndmask_b32 v34, v5, v76, s[72:73]\n"
    "v_cndmask_b32 v35, v5, v78, s[72:73]\n"
    "v_mfma_f32_32x32x16_bf16 v[64:79], v[28:31], v[96:99], 0\n"
    "v_cndmask_b32 v36, v5, v80, s[72:73]\n"
    "v_cndmask_b32 v37, v5, v82, s[72:73]\n"
    "v_cndmask_b32 v38, v5, v84, s[72:73]\n"
    "v_cndmask_b32 v39, v5, v86, s[72:73]\n"
    "s_mov_b32 s63, 5\n"
    "s_mov_b32 s67, 100000\n"
    "s_nop 5\n"
    "v_exp_f32 v64, v64\n"
    "v_exp_f32 v65, v65\n"
    "v_exp_f32 v66, v66\n"
    "v_exp_f32 v67, v67\n"
    "v_exp_f32 v68, v68\n"
    "v_mfma_f32_32x32x16_bf16 v[80:95], v[28:31], v[100:103], 0\n"
    "v_exp_f32 v69, v69\n"
    "v_exp_f32 v70, v70\n"
    "v_exp_f32 v71, v71\n"
    "v_exp_f32 v72, v72\n"
    "v_exp_f32 v73, v73\n"
    "v_exp_f32 v74, v74\n"
    "v_exp_f32 v75, v75\n"
    "v_exp_f32 v76, v76\n"
    "v_exp_f32 v77, v77\n"
    "v_exp_f32 v78, v78\n"
    "v_smfmac_f32_32x32x32_bf16 v[128:143], v[32:35], v[64:71], v3\n"
    "v_exp_f32 v79, v79\n"
    "v_exp_f32 v80, v80\n"
    "v_exp_f32 v81, v81\n"
    "v_smfmac_f32_32x32x32_bf16 v[128:143], v[36:39], v[72:79], v3\n"
    "v_exp_f32 v82, v82\n"
    "v_exp_f32 v83, v83\n"
    "v_exp_f32 v84, v84\n"
    "v_mfma_f32_32x32x16_bf16 v[64:79], v[28:31], v[104:107], 0\n"
    "v_exp_f32 v85, v85\n"
    "v_exp_f32 v86, v86\n"
    "v_exp_f32 v87, v87\n"
    "v_exp_f32 v88, v88\n"
    "v_exp_f32 v89, v89\n"
    "v_exp_f32 v90, v90\n"
    "v_exp_f32 v91, v91\n"
    "v_exp_f32 v92, v92\n"
    "v_exp_f32 v93, v93\n"
    "v_exp_f32 v94, v94\n"
    "v_smfmac_f32_32x32x32_bf16 v[144:159], v[32:35], v[80:87], v3\n"
    "v_exp_f32 v95, v95\n"
    "v_exp_f32 v64, v64\n"
    "v_exp_f32 v65, v65\n"
    "v_smfmac_f32_32x32x32_bf16 v[144:159], v[36:39], v[88:95], v3\n"
    ".LA_kvchk:\n"
    "s_waitcnt vmcnt(0)\n"
    "v_cmp_ne_u64 vcc, s[56:57], v[62:63]\n"
    "s_and_b64 s[60:61], vcc, s[70:71]\n"
    "s_cbranch_scc1 .LA_kvwait\n"
    ".LA_kvok:\n"
    "s_or_b32 s64, vcc_lo, vcc_hi\n"
    "v_exp_f32 v66, v66\n"
    "v_exp_f32 v67, v67\n"
    "v_exp_f32 v68, v68\n"
    "v_mfma_f32_32x32x16_bf16 v[80:95], v[28:31], v[108:111], 0\n"
    "buffer_load_dwordx4 v[40:43], v10, s[28:31], s92 offen sc1\n"
    "v_exp_f32 v69, v69\n"
    "v_exp_f32 v70, v70\n"
    "v_exp_f32 v71, v71\n"
    "v_exp_f32 v72, v72\n"
    "v_exp_f32 v73, v73\n"
    "v_exp_f32 v74, v74\n"
    "v_exp_f32 v75, v75\n"
    "v_exp_f32 v76, v76\n"
    "v_exp_f32 v77, v77\n"
    "v_exp_f32 v78, v78\n"
    "v_smfmac_f32_32x32x32_bf16 v[160:175], v[32:35], v[64:71], v3\n"
    "buffer_load_dwordx4 v[44:47], v2, s[28:31], s92 offen offset:1024 sc1\n"
    "v_exp_f32 v79, v79\n"
    "v_exp_f32 v80, v80\n"
    "v_exp_f32 v81, v81\n"
    "v_smfmac_f32_32x32x32_bf16 v[160:175], v[36:39], v[72:79], v3\n"
    "buffer_load_dwordx4 v[48:51], v2, s[28:31], s92 offen offset:1664 sc1\n"
    "v_exp_f32 v82, v82\n"
    "v_exp_f32 v83, v83\n"
    "v_exp_f32 v84, v84\n"
    "v_mfma_f32_32x32x16_bf16 v[64:79], v[28:31], v[112:115], 0\n"
    "s_add_u32 s94, s94, 1\n"
    "s_xor_b32 s92, s94, s53\n"
    "v_exp_f32 v85, v85\n"
    "v_exp_f32 v86, v86\n"
    "v_exp_f32 v87, v87\n"
    "v_exp_f32 v88, v88\n"
    "v_exp_f32 v89, v89\n"
    "v_exp_f32 v90, v90\n"
    "v_exp_f32 v91, v91\n"
    "v_exp_f32 v92, v92\n"
    "v_exp_f32 v93, v93\n"
    "v_exp_f32 v94, v94\n"
    "v_smfmac_f32_32x32x32_bf16 v[176:191], v[32:35], v[80:87], v3\n"
    "s_mul_i32 s92, s92, 2304\n"
    "s_add_u32 s92, s92, s93\n"
    "v_exp_f32 v95, v95\n"
    "v_exp_f32 v64, v64\n"
    "v_exp_f32 v65, v65\n"
    "v_smfmac_f32_32x32x32_bf16 v[176:191], v[36:39], v[88:95], v3\n"
    "buffer_load_dwordx4 v[52:55], v10, s[28:31], s92 offen sc1\n"
    "v_exp_f32 v66, v66\n"
    "v_exp_f32 v67, v67\n"
    "v_exp_f32 v68, v68\n"
    "v_mfma_f32_32x32x16_bf16 v[80:95], v[28:31], v[116:119], 0\n"
    "buffer_load_dwordx4 v[56:59], v2, s[28:31], s92 offen offset:1024 sc1\n"
    "v_exp_f32 v69, v69\n"
    "v_exp_f32 v70, v70\n"
    "v_exp_f32 v71, v71\n"
    "v_exp_f32 v72, v72\n"
    "v_exp_f32 v73, v73\n"
    "v_exp_f32 v74, v74\n"
    "v_exp_f32 v75, v75\n"
    "v_exp_f32 v76, v76\n"
    "v_exp_f32 v77, v77\n"
    "v_exp_f32 v78, v78\n"
    "v_smfmac_f32_32x32x32_bf16 v[192:207], v[32:35], v[64:71], v3\n"
    "buffer_load_dwordx4 v[60:63], v2, s[28:31], s92 offen offset:1664 sc1\n"
    "v_exp_f32 v79, v79\n"
    "v_exp_f32 v80, v80\n"
    "v_exp_f32 v81, v81\n"
    "v_smfmac_f32_32x32x32_bf16 v[192:207], v[36:39], v[72:79], v3\n"
    "s_add_u32 s94, s94, 1\n"
    "s_xor_b32 s92, s94, s53\n"
    "v_exp_f32 v82, v82\n"
    "v_exp_f32 v83, v83\n"
    "v_exp_f32 v84, v84\n"
    "v_mfma_f32_32x32x16_bf16 v[64:79], v[28:31], v[120:123], 0\n"
    "s_mul_i32 s92, s92, 2304\n"
    "s_add_u32 s92, s92, s93\n"
    "v_exp_f32 v85, v85\n"
    "v_exp_f32 v86, v86\n"
    "v_exp_f32 v87, v87\n"
    "v_exp_f32 v88, v88\n"
    "v_exp_f32 v89, v89\n"
    "v_exp_f32 v90, v90\n"
    "v_exp_f32 v91, v91\n"
    "v_exp_f32 v92, v92\n"
    "v_exp_f32 v93, v93\n"
    "v_exp_f32 v94, v94\n"
    "v_smfmac_f32_32x32x32_bf16 v[208:223], v[32:35], v[80:87], v3\n"
    "buffer_store_dwordx4 v[16:19], v10, s[28:31], s90 offen sc1\n"
    "v_exp_f32 v95, v95\n"
    "v_exp_f32 v64, v64\n"
    "v_exp_f32 v65, v65\n"
    "v_smfmac_f32_32x32x32_bf16 v[208:223], v[36:39], v[88:95], v3\n"
    "buffer_store_dwordx4 v[16:19], v10, s[28:31], s90 offen offset:1024 sc1\n"
    "v_exp_f32 v66, v66\n"
    "v_exp_f32 v67, v67\n"
    "v_exp_f32 v68, v68\n"
    "v_mfma_f32_32x32x16_bf16 v[80:95], v[28:31], v[124:127], 0\n"
    "v_exp_f32 v69, v69\n"
    "v_exp_f32 v70, v70\n"
    "v_exp_f32 v71, v71\n"
    "v_exp_f32 v72, v72\n"
    "v_exp_f32 v73, v73\n"
    "v_exp_f32 v74, v74\n"
    "v_exp_f32 v75, v75\n"
    "v_exp_f32 v76, v76\n"
    "v_exp_f32 v77, v77\n"
    "v_exp_f32 v78, v78\n"
    "v_smfmac_f32_32x32x32_bf16 v[224:239], v[32:35], v[64:71], v3\n"
    "v_exp_f32 v79, v79\n"
    "v_exp_f32 v80, v80\n"
    "v_exp_f32 v81, v81\n"
    "v_smfmac_f32_32x32x32_bf16 v[224:239], v[36:39], v[72:79], v3\n"
    "s_waitcnt vmcnt(5)\n"
    "v_exp_f32 v82, v82\n"
    "v_exp_f32 v83, v83\n"
    "v_exp_f32 v84, v84\n"
    "v_mfma_f32_32x32x16_bf16 v[64:79], v[40:43], v[96:99], 0\n"
    "v_exp_f32 v85, v85\n"
    "v_exp_f32 v86, v86\n"
    "v_exp_f32 v87, v87\n"
    "v_exp_f32 v88, v88\n"
    "v_exp_f32 v89, v89\n"
    "v_exp_f32 v90, v90\n"
    "v_exp_f32 v91, v91\n"
    "v_exp_f32 v92, v92\n"
    "v_exp_f32 v93, v93\n"
    "v_exp_f32 v94, v94\n"
    "v_smfmac_f32_32x32x32_bf16 v[240:255], v[32:35], v[80:87], v3\n"
    "v_exp_f32 v95, v95\n"
    "s_branch .LA_entry\n"
    ".LA_loop:\n"
    "v_exp_f32 v64, v64\n"
    "v_exp_f32 v65, v65\n"
    "v_smfmac_f32_32x32x32_bf16 v[240:255], v[60:63], v[88:95], v3\n"
    "v_exp_f32 v66, v66\n"
    "v_exp_f32 v67, v67\n"
    "v_exp_f32 v68, v68\n"
    "v_mfma_f32_32x32x16_bf16 v[80:95], v[28:31], v[100:103], 0\n"
    "buffer_load_dwordx4 v[52:55], v10, s[28:31], s92 offen sc1\n"
    "v_exp_f32 v69, v69\n"
    "v_exp_f32 v70, v70\n"
    "v_exp_f32 v71, v71\n"
    "v_exp_f32 v72, v72\n"
    "v_exp_f32 v73, v73\n"
    "v_exp_f32 v74, v74\n"
    "v_exp_f32 v75, v75\n"
    "v_exp_f32 v76, v76\n"
    "v_exp_f32 v77, v77\n"
    "v_exp_f32 v78, v78\n"
    "v_smfmac_f32_32x32x32_bf16 v[128:143], v[32:35], v[64:71], v3\n"
    "buffer_load_dwordx4 v[56:59], v2, s[28:31], s92 offen offset:1024 sc1\n"
    "v_exp_f32 v79, v79\n"
    "v_exp_f32 v80, v80\n"
    "v_exp_f32 v81, v81\n"
    "v_smfmac_f32_32x32x32_bf16 v[128:143], v[36:39], v[72:79], v3\n"
    "buffer_load_dwordx4 v[60:63], v2, s[28:31], s92 offen offset:1664 sc1\n"
    "v_exp_f32 v82, v82\n"
    "v_exp_f32 v83, v83\n"
    "v_exp_f32 v84, v84\n"
    "v_mfma_f32_32x32x16_bf16 v[64:79], v[28:31], v[104:107], 0\n"
    "s_add_u32 s94, s94, 1\n"
    "v_exp_f32 v85, v85\n"
    "v_exp_f32 v86, v86\n"
    "v_exp_f32 v87, v87\n"
    "v_exp_f32 v88, v88\n"
    "v_exp_f32 v89, v89\n"
    "v_exp_f32 v90, v90\n"
    "v_exp_f32 v91, v91\n"
    "v_exp_f32 v92, v92\n"
    "v_exp_f32 v93, v93\n"
    "v_exp_f32 v94, v94\n"
    "v_smfmac_f32_32x32x32_bf16 v[144:159], v[32:35], v[80:87], v3\n"
    "s_xor_b32 s92, s94, s53\n"
    "v_exp_f32 v95, v95\n"
    "v_exp_f32 v64, v64\n"
    "v_exp_f32 v65, v65\n"
    "v_smfmac_f32_32x32x32_bf16 v[144:159], v[36:39], v[88:95], v3\n"
    "s_mul_i32 s92, s92, 2304\n"
    "v_exp_f32 v66, v66\n"
    "v_exp_f32 v67, v67\n"
    "v_exp_f32 v68, v68\n"
    "v_mfma_f32_32x32x16_bf16 v[80:95], v[28:31], v[108:111], 0\n"
    "s_add_u32 s92, s92, s93\n"
    "v_exp_f32 v69, v69\n"
    "v_exp_f32 v70, v70\n"
    "v_exp_f32 v71, v71\n"
    "v_exp_f32 v72, v72\n"
    "v_exp_f32 v73, v73\n"
    "v_exp_f32 v74, v74\n"
    "v_exp_f32 v75, v75\n"
    "v_exp_f32 v76, v76\n"
    "v_exp_f32 v77, v77\n"
    "v_exp_f32 v78, v78\n"
    "v_smfmac_f32_32x32x32_bf16 v[160:175], v[32:35], v[64:71], v3\n"
    "v_exp_f32 v79, v79\n"
    "v_exp_f32 v80, v80\n"
    "v_exp_f32 v81, v81\n"
    "v_smfmac_f32_32x32x32_bf16 v[160:175], v[36:39], v[72:79], v3\n"
    "v_exp_f32 v82, v82\n"
    "v_exp_f32 v83, v83\n"
    "v_exp_f32 v84, v84\n"
    "v_mfma_f32_32x32x16_bf16 v[64:79], v[28:31], v[112:115], 0\n"
    "v_exp_f32 v85, v85\n"
    "v_exp_f32 v86, v86\n"
    "v_exp_f32 v87, v87\n"
    "v_exp_f32 v88, v88\n"
    "v_exp_f32 v89, v89\n"
    "v_exp_f32 v90, v90\n"
    "v_exp_f32 v91, v91\n"
    "v_exp_f32 v92, v92\n"
    "v_exp_f32 v93, v93\n"
    "v_exp_f32 v94, v94\n"
    "v_smfmac_f32_32x32x32_bf16 v[176:191], v[32:35], v[80:87], v3\n"
    "v_exp_f32 v95, v95\n"
    "v_exp_f32 v64, v64\n"
    "v_exp_f32 v65, v65\n"
    "v_smfmac_f32_32x32x32_bf16 v[176:191], v[36:39], v[88:95], v3\n"
    "v_exp_f32 v66, v66\n"
    "v_exp_f32 v67, v67\n"
    "v_exp_f32 v68, v68\n"
    "v_mfma_f32_32x32x16_bf16 v[80:95], v[28:31], v[116:119], 0\n"
    "v_exp_f32 v69, v69\n"
    "v_exp_f32 v70, v70\n"
    "v_exp_f32 v71, v71\n"
    "v_exp_f32 v72, v72\n"
    "v_exp_f32 v73, v73\n"
    "v_exp_f32 v74, v74\n"
    "v_exp_f32 v75, v75\n"
    "v_exp_f32 v76, v76\n"
    "v_exp_f32 v77, v77\n"
    "v_exp_f32 v78, v78\n"
    "v_smfmac_f32_32x32x32_bf16 v[192:207], v[32:35], v[64:71], v3\n"
    "v_exp_f32 v79, v79\n"
    "v_exp_f32 v80, v80\n"
    "v_exp_f32 v81, v81\n"
    "v_smfmac_f32_32x32x32_bf16 v[192:207], v[36:39], v[72:79], v3\n"
    "v_exp_f32 v82, v82\n"
    "v_exp_f32 v83, v83\n"
    "v_exp_f32 v84, v84\n"
    "v_mfma_f32_32x32x16_bf16 v[64:79], v[28:31], v[120:123], 0\n"
    "v_exp_f32 v85, v85\n"
    "v_exp_f32 v86, v86\n"
    "v_exp_f32 v87, v87\n"
    "v_exp_f32 v88, v88\n"
    "v_exp_f32 v89, v89\n"
    "v_exp_f32 v90, v90\n"
    "v_exp_f32 v91, v91\n"
    "v_exp_f32 v92, v92\n"
    "v_exp_f32 v93, v93\n"
    "v_exp_f32 v94, v94\n"
    "v_smfmac_f32_32x32x32_bf16 v[208:223], v[32:35], v[80:87], v3\n"
    "v_exp_f32 v95, v95\n"
    "v_exp_f32 v64, v64\n"
    "v_exp_f32 v65, v65\n"
    "v_smfmac_f32_32x32x32_bf16 v[208:223], v[36:39], v[88:95], v3\n"
    "v_exp_f32 v66, v66\n"
    "v_exp_f32 v67, v67\n"
    "v_exp_f32 v68, v68\n"
    "v_mfma_f32_32x32x16_bf16 v[80:95], v[28:31], v[124:127], 0\n"
    "v_exp_f32 v69, v69\n"
    "v_exp_f32 v70, v70\n"
    "v_exp_f32 v71, v71\n"
    "v_exp_f32 v72, v72\n"
    "v_exp_f32 v73, v73\n"
    "v_exp_f32 v74, v74\n"
    "v_exp_f32 v75, v75\n"
    "v_exp_f32 v76, v76\n"
    "v_exp_f32 v77, v77\n"
    "v_exp_f32 v78, v78\n"
    "v_smfmac_f32_32x32x32_bf16 v[224:239], v[32:35], v[64:71], v3\n"
    "v_exp_f32 v79, v79\n"
    "v_exp_f32 v80, v80\n"
    "v_exp_f32 v81, v81\n"
    "v_smfmac_f32_32x32x32_bf16 v[224:239], v[36:39], v[72:79], v3\n"
    "s_waitcnt vmcnt(3)\n"
    "v_exp_f32 v82, v82\n"
    "v_exp_f32 v83, v83\n"
    "v_exp_f32 v84, v84\n"
    "v_mfma_f32_32x32x16_bf16 v[64:79], v[40:43], v[96:99], 0\n"
    "v_exp_f32 v85, v85\n"
    "v_exp_f32 v86, v86\n"
    "v_exp_f32 v87, v87\n"
    "v_exp_f32 v88, v88\n"
    "v_exp_f32 v89, v89\n"
    "v_exp_f32 v90, v90\n"
    "v_exp_f32 v91, v91\n"
    "v_exp_f32 v92, v92\n"
    "v_exp_f32 v93, v93\n"
    "v_exp_f32 v94, v94\n"
    "v_smfmac_f32_32x32x32_bf16 v[240:255], v[32:35], v[80:87], v3\n"
    "v_exp_f32 v95, v95\n"
    ".LA_entry:\n"
    "v_exp_f32 v64, v64\n"
    "v_exp_f32 v65, v65\n"
    "v_smfmac_f32_32x32x32_bf16 v[240:255], v[36:39], v[88:95], v3\n"
    "v_exp_f32 v66, v66\n"
    "v_exp_f32 v67, v67\n"
    "v_exp_f32 v68, v68\n"
    "v_mfma_f32_32x32x16_bf16 v[80:95], v[40:43], v[100:103], 0\n"
    "buffer_load_dwordx4 v[28:31], v10, s[28:31], s92 offen sc1\n"
    "v_exp_f32 v69, v69\n"
    "v_exp_f32 v70, v70\n"
    "v_exp_f32 v71, v71\n"
    "v_exp_f32 v72, v72\n"
    "v_exp_f32 v73, v73\n"
    "v_exp_f32 v74, v74\n"
    "v_exp_f32 v75, v75\n"
    "v_exp_f32 v76, v76\n"
    "v_exp_f32 v77, v77\n"
    "v_exp_f32 v78, v78\n"
    "v_smfmac_f32_32x32x32_bf16 v[128:143], v[44:47], v[64:71], v3\n"
    "buffer_load_dwordx4 v[32:35], v2, s[28:31], s92 offen offset:1024 sc1\n"
    "v_exp_f32 v79, v79\n"
    "v_exp_f32 v80, v80\n"
    "v_exp_f32 v81, v81\n"
    "v_smfmac_f32_32x32x32_bf16 v[128:143], v[48:51], v[72:79], v3\n"
    "buffer_load_dwordx4 v[36:39], v2, s[28:31], s92 offen offset:1664 sc1\n"
    "v_exp_f32 v82, v82\n"
    "v_exp_f32 v83, v83\n"
    "v_exp_f32 v84, v84\n"
    "v_mfma_f32_32x32x16_bf16 v[64:79], v[40:43], v[104:107], 0\n"
    "s_add_u32 s94, s94, 1\n"
    "v_exp_f32 v85, v85\n"
    "v_exp_f32 v86, v86\n"
    "v_exp_f32 v87, v87\n"
    "v_exp_f32 v88, v88\n"
    "v_exp_f32 v89, v89\n"
    "v_exp_f32 v90, v90\n"
    "v_exp_f32 v91, v91\n"
    "v_exp_f32 v92, v92\n"
    "v_exp_f32 v93, v93\n"
    "v_exp_f32 v94, v94\n"
    "v_smfmac_f32_32x32x32_bf16 v[144:159], v[44:47], v[80:87], v3\n"
    "s_xor_b32 s92, s94, s53\n"
    "v_exp_f32 v95, v95\n"
    "v_exp_f32 v64, v64\n"
    "v_exp_f32 v65, v65\n"
    "v_smfmac_f32_32x32x32_bf16 v[144:159], v[48:51], v[88:95], v3\n"
    "s_mul_i32 s92, s92, 2304\n"
    "v_exp_f32 v66, v66\n"
    "v_exp_f32 v67, v67\n"
    "v_exp_f32 v68, v68\n"
    "v_mfma_f32_32x32x16_bf16 v[80:95], v[40:43], v[108:111], 0\n"
    "s_add_u32 s92, s92, s93\n"
    "v_exp_f32 v69, v69\n"
    "v_exp_f32 v70, v70\n"
    "v_exp_f32 v71, v71\n"
    "v_exp_f32 v72, v72\n"
    "v_exp_f32 v73, v73\n"
    "v_exp_f32 v74, v74\n"
    "v_exp_f32 v75, v75\n"
    "v_exp_f32 v76, v76\n"
    "v_exp_f32 v77, v77\n"
    "v_exp_f32 v78, v78\n"
    "v_smfmac_f32_32x32x32_bf16 v[160:175], v[44:47], v[64:71], v3\n"
    "v_exp_f32 v79, v79\n"
    "v_exp_f32 v80, v80\n"
    "v_exp_f32 v81, v81\n"
    "v_smfmac_f32_32x32x32_bf16 v[160:175], v[48:51], v[72:79], v3\n"
    "v_exp_f32 v82, v82\n"
    "v_exp_f32 v83, v83\n"
    "v_exp_f32 v84, v84\n"
    "v_mfma_f32_32x32x16_bf16 v[64:79], v[40:43], v[112:115], 0\n"
    "v_exp_f32 v85, v85\n"
    "v_exp_f32 v86, v86\n"
    "v_exp_f32 v87, v87\n"
    "v_exp_f32 v88, v88\n"
    "v_exp_f32 v89, v89\n"
    "v_exp_f32 v90, v90\n"
    "v_exp_f32 v91, v91\n"
    "v_exp_f32 v92, v92\n"
    "v_exp_f32 v93, v93\n"
    "v_exp_f32 v94, v94\n"
    "v_smfmac_f32_32x32x32_bf16 v[176:191], v[44:47], v[80:87], v3\n"
    "v_exp_f32 v95, v95\n"
    "v_exp_f32 v64, v64\n"
    "v_exp_f32 v65, v65\n"
    "v_smfmac_f32_32x32x32_bf16 v[176:191], v[48:51], v[88:95], v3\n"
    "v_exp_f32 v66, v66\n"
    "v_exp_f32 v67, v67\n"
    "v_exp_f32 v68, v68\n"
    "v_mfma_f32_32x32x16_bf16 v[80:95], v[40:43], v[116:119], 0\n"
    "v_exp_f32 v69, v69\n"
    "v_exp_f32 v70, v70\n"
    "v_exp_f32 v71, v71\n"
    "v_exp_f32 v72, v72\n"
    "v_exp_f32 v73, v73\n"
    "v_exp_f32 v74, v74\n"
    "v_exp_f32 v75, v75\n"
    "v_exp_f32 v76, v76\n"
    "v_exp_f32 v77, v77\n"
    "v_exp_f32 v78, v78\n"
    "v_smfmac_f32_32x32x32_bf16 v[192:207], v[44:47], v[64:71], v3\n"
    "v_exp_f32 v79, v79\n"
    "v_exp_f32 v80, v80\n"
    "v_exp_f32 v81, v81\n"
    "v_smfmac_f32_32x32x32_bf16 v[192:207], v[48:51], v[72:79], v3\n"
    "v_exp_f32 v82, v82\n"
    "v_exp_f32 v83, v83\n"
    "v_exp_f32 v84, v84\n"
    "v_mfma_f32_32x32x16_bf16 v[64:79], v[40:43], v[120:123], 0\n"
    "v_exp_f32 v85, v85\n"
    "v_exp_f32 v86, v86\n"
    "v_exp_f32 v87, v87\n"
    "v_exp_f32 v88, v88\n"
    "v_exp_f32 v89, v89\n"
    "v_exp_f32 v90, v90\n"
    "v_exp_f32 v91, v91\n"
    "v_exp_f32 v92, v92\n"
    "v_exp_f32 v93, v93\n"
    "v_exp_f32 v94, v94\n"
    "v_smfmac_f32_32x32x32_bf16 v[208:223], v[44:47], v[80:87], v3\n"
    "v_exp_f32 v95, v95\n"
    "v_exp_f32 v64, v64\n"
    "v_exp_f32 v65, v65\n"
    "v_smfmac_f32_32x32x32_bf16 v[208:223], v[48:51], v[88:95], v3\n"
    "v_exp_f32 v66, v66\n"
    "v_exp_f32 v67, v67\n"
    "v_exp_f32 v68, v68\n"
    "v_mfma_f32_32x32x16_bf16 v[80:95], v[40:43], v[124:127], 0\n"
    "v_exp_f32 v69, v69\n"
    "v_exp_f32 v70, v70\n"
    "v_exp_f32 v71, v71\n"
    "v_exp_f32 v72, v72\n"
    "v_exp_f32 v73, v73\n"
    "v_exp_f32 v74, v74\n"
    "v_exp_f32 v75, v75\n"
    "v_exp_f32 v76, v76\n"
    "v_exp_f32 v77, v77\n"
    "v_exp_f32 v78, v78\n"
    "v_smfmac_f32_32x32x32_bf16 v[224:239], v[44:47], v[64:71], v3\n"
    "v_exp_f32 v79, v79\n"
    "v_exp_f32 v80, v80\n"
    "v_exp_f32 v81, v81\n"
    "v_smfmac_f32_32x32x32_bf16 v[224:239], v[48:51], v[72:79], v3\n"
    "s_waitcnt vmcnt(3)\n"
    "v_exp_f32 v82, v82\n"
    "v_exp_f32 v83, v83\n"
    "v_exp_f32 v84, v84\n"
    "v_mfma_f32_32x32x16_bf16 v[64:79], v[52:55], v[96:99], 0\n"
    "s_cmp_lg_u32 s64, 0\n"
    "v_exp_f32 v85, v85\n"
    "v_exp_f32 v86, v86\n"
    "v_exp_f32 v87, v87\n"
    "v_exp_f32 v88, v88\n"
    "v_exp_f32 v89, v89\n"
    "v_exp_f32 v90, v90\n"
    "v_exp_f32 v91, v91\n"
    "v_exp_f32 v92, v92\n"
    "v_exp_f32 v93, v93\n"
    "v_exp_f32 v94, v94\n"
    "v_smfmac_f32_32x32x32_bf16 v[240:255], v[44:47], v[80:87], v3\n"
    "s_cbranch_scc1 .LA_kvrest\n"
    ".LA_kvall:\n"
    "v_exp_f32 v95, v95\n"
    "v_exp_f32 v64, v64\n"
    "v_exp_f32 v65, v65\n"
    "v_smfmac_f32_32x32x32_bf16 v[240:255], v[48:51], v[88:95], v3\n"
    "v_exp_f32 v66, v66\n"
    "v_exp_f32 v67, v67\n"
    "v_exp_f32 v68, v68\n"
    "v_mfma_f32_32x32x16_bf16 v[80:95], v[52:55], v[100:103], 0\n"
    "buffer_load_dwordx4 v[40:43], v10, s[28:31], s92 offen sc1\n"
    "v_exp_f32 v69, v69\n"
    "v_exp_f32 v70, v70\n"
    "v_exp_f32 v71, v71\n"
    "v_exp_f32 v72, v72\n"
    "v_exp_f32 v73, v73\n"
    "v_exp_f32 v74, v74\n"
    "v_exp_f32 v75, v75\n"
    "v_exp_f32 v76, v76\n"
    "v_exp_f32 v77, v77\n"
    "v_exp_f32 v78, v78\n"
    "v_smfmac_f32_32x32x32_bf16 v[128:143], v[56:59], v[64:71], v3\n"
    "buffer_load_dwordx4 v[44:47], v2, s[28:31], s92 offen offset:1024 sc1\n"
    "v_exp_f32 v79, v79\n"
    "v_exp_f32 v80, v80\n"
    "v_exp_f32 v81, v81\n"
    "v_smfmac_f32_32x32x32_bf16 v[128:143], v[60:63], v[72:79], v3\n"
    "buffer_load_dwordx4 v[48:51], v2, s[28:31], s92 offen offset:1664 sc1\n"
    "v_exp_f32 v82, v82\n"
    "v_exp_f32 v83, v83\n"
    "v_exp_f32 v84, v84\n"
    "v_mfma_f32_32x32x16_bf16 v[64:79], v[52:55], v[104:107], 0\n"
    "s_add_u32 s94, s94, 1\n"
    "v_exp_f32 v85, v85\n"
    "v_exp_f32 v86, v86\n"
    "v_exp_f32 v87, v87\n"
    "v_exp_f32 v88, v88\n"
    "v_exp_f32 v89, v89\n"
    "v_exp_f32 v90, v90\n"
    "v_exp_f32 v91, v91\n"
    "v_exp_f32 v92, v92\n"
    "v_exp_f32 v93, v93\n"
    "v_exp_f32 v94, v94\n"
    "v_smfmac_f32_32x32x32_bf16 v[144:159], v[56:59], v[80:87], v3\n"
    "s_xor_b32 s92, s94, s53\n"
    "v_exp_f32 v95, v95\n"
    "v_exp_f32 v64, v64\n"
    "v_exp_f32 v65, v65\n"
    "v_smfmac_f32_32x32x32_bf16 v[144:159], v[60:63], v[88:95], v3\n"
    "s_mul_i32 s92, s92, 2304\n"
    "v_exp_f32 v66, v66\n"
    "v_exp_f32 v67, v67\n"
    "v_exp_f32 v68, v68\n"
    "v_mfma_f32_32x32x16_bf16 v[80:95], v[52:55], v[108:111], 0\n"
    "s_add_u32 s92, s92, s93\n"
    "v_exp_f32 v69, v69\n"
    "v_exp_f32 v70, v70\n"
    "v_exp_f32 v71, v71\n"
    "v_exp_f32 v72, v72\n"
    "v_exp_f32 v73, v73\n"
    "v_exp_f32 v74, v74\n"
    "v_exp_f32 v75, v75\n"
    "v_exp_f32 v76, v76\n"
    "v_exp_f32 v77, v77\n"
    "v_exp_f32 v78, v78\n"
    "v_smfmac_f32_32x32x32_bf16 v[160:175], v[56:59], v[64:71], v3\n"
    "v_exp_f32 v79, v79\n"
    "v_exp_f32 v80, v80\n"
    "v_exp_f32 v81, v81\n"
    "v_smfmac_f32_32x32x32_bf16 v[160:175], v[60:63], v[72:79], v3\n"
    "v_exp_f32 v82, v82\n"
    "v_exp_f32 v83, v83\n"
    "v_exp_f32 v84, v84\n"
    "v_mfma_f32_32x32x16_bf16 v[64:79], v[52:55], v[112:115], 0\n"
    "v_exp_f32 v85, v85\n"
    "v_exp_f32 v86, v86\n"
    "v_exp_f32 v87, v87\n"
    "v_exp_f32 v88, v88\n"
    "v_exp_f32 v89, v89\n"
    "v_exp_f32 v90, v90\n"
    "v_exp_f32 v91, v91\n"
    "v_exp_f32 v92, v92\n"
    "v_exp_f32 v93, v93\n"
    "v_exp_f32 v94, v94\n"
    "v_smfmac_f32_32x32x32_bf16 v[176:191], v[56:59], v[80:87], v3\n"
    "v_exp_f32 v95, v95\n"
    "v_exp_f32 v64, v64\n"
    "v_exp_f32 v65, v65\n"
    "v_smfmac_f32_32x32x32_bf16 v[176:191], v[60:63], v[88:95], v3\n"
    "v_exp_f32 v66, v66\n"
    "v_exp_f32 v67, v67\n"
    "v_exp_f32 v68, v68\n"
    "v_mfma_f32_32x32x16_bf16 v[80:95], v[52:55], v[116:119], 0\n"
    "v_exp_f32 v69, v69\n"
    "v_exp_f32 v70, v70\n"
    "v_exp_f32 v71, v71\n"
    "v_exp_f32 v72, v72\n"
    "v_exp_f32 v73, v73\n"
    "v_exp_f32 v74, v74\n"
    "v_exp_f32 v75, v75\n"
    "v_exp_f32 v76, v76\n"
    "v_exp_f32 v77, v77\n"
    "v_exp_f32 v78, v78\n"
    "v_smfmac_f32_32x32x32_bf16 v[192:207], v[56:59], v[64:71], v3\n"
    "v_exp_f32 v79, v79\n"
    "v_exp_f32 v80, v80\n"
    "v_exp_f32 v81, v81\n"
    "v_smfmac_f32_32x32x32_bf16 v[192:207], v[60:63], v[72:79], v3\n"
    "v_exp_f32 v82, v82\n"
    "v_exp_f32 v83, v83\n"
    "v_exp_f32 v84, v84\n"
    "v_mfma_f32_32x32x16_bf16 v[64:79], v[52:55], v[120:123], 0\n"
    "v_exp_f32 v85, v85\n"
    "v_exp_f32 v86, v86\n"
    "v_exp_f32 v87, v87\n"
    "v_exp_f32 v88, v88\n"
    "v_exp_f32 v89, v89\n"
    "v_exp_f32 v90, v90\n"
    "v_exp_f32 v91, v91\n"
    "v_exp_f32 v92, v92\n"
    "v_exp_f32 v93, v93\n"
    "v_exp_f32 v94, v94\n"
    "v_smfmac_f32_32x32x32_bf16 v[208:223], v[56:59], v[80:87], v3\n"
    "v_exp_f32 v95, v95\n"
    "v_exp_f32 v64, v64\n"
    "v_exp_f32 v65, v65\n"
    "v_smfmac_f32_32x32x32_bf16 v[208:223], v[60:63], v[88:95], v3\n"
    "s_sub_u32 s63, s63, 1\n"
    "v_exp_f32 v66, v66\n"
    "v_exp_f32 v67, v67\n"
    "v_exp_f32 v68, v68\n"
    "v_mfma_f32_32x32x16_bf16 v[80:95], v[52:55], v[124:127], 0\n"
    "s_cmp_lg_u32 s63, 0\n"
    "v_exp_f32 v69, v69\n"
    "v_exp_f32 v70, v70\n"
    "v_exp_f32 v71, v71\n"
    "v_exp_f32 v72, v72\n"
    "v_exp_f32 v73, v73\n"
    "v_exp_f32 v74, v74\n"
    "v_exp_f32 v75, v75\n"
    "v_exp_f32 v76, v76\n"
    "v_exp_f32 v77, v77\n"
    "v_exp_f32 v78, v78\n"
    "v_smfmac_f32_32x32x32_bf16 v[224:239], v[56:59], v[64:71], v3\n"
    "v_exp_f32 v79, v79\n"
    "v_exp_f32 v80, v80\n"
    "v_exp_f32 v81, v81\n"
    "v_smfmac_f32_32x32x32_bf16 v[224:239], v[60:63], v[72:79], v3\n"
    "s_waitcnt vmcnt(3)\n"
    "v_exp_f32 v82, v82\n"
    "v_exp_f32 v83, v83\n"
    "v_exp_f32 v84, v84\n"
    "v_mfma_f32_32x32x16_bf16 v[64:79], v[28:31], v[96:99], 0\n"
    "v_exp_f32 v85, v85\n"
    "v_exp_f32 v86, v86\n"
    "v_exp_f32 v87, v87\n"
    "v_exp_f32 v88, v88\n"
    "v_exp_f32 v89, v89\n"
    "v_exp_f32 v90, v90\n"
    "v_exp_f32 v91, v91\n"
    "v_exp_f32 v92, v92\n"
    "v_exp_f32 v93, v93\n"
    "v_exp_f32 v94, v94\n"
    "v_smfmac_f32_32x32x32_bf16 v[240:255], v[56:59], v[80:87], v3\n"
    "v_exp_f32 v95, v95\n"
    "s_cbranch_scc1 .LA_loop\n"
    "v_exp_f32 v64, v64\n"
    "v_exp_f32 v65, v65\n"
    "v_smfmac_f32_32x32x32_bf16 v[240:255], v[60:63], v[88:95], v3\n"
    "ds_read_b32 v13, v14 offset:8\n"
    "v_exp_f32 v66, v66\n"
    "v_exp_f32 v67, v67\n"
    "v_exp_f32 v68, v68\n"
    "v_mfma_f32_32x32x16_bf16 v[80:95], v[28:31], v[100:103], 0\n"
    "v_exp_f32 v69, v69\n"
    "v_exp_f32 v70, v70\n"
    "v_exp_f32 v71, v71\n"
    "v_exp_f32 v72, v72\n"
    "v_exp_f32 v73, v73\n"
    "v_exp_f32 v74, v74\n"
    "v_exp_f32 v75, v75\n"
    "v_exp_f32 v76, v76\n"
    "v_exp_f32 v77, v77\n"
    "v_exp_f32 v78, v78\n"
    "v_smfmac_f32_32x32x32_bf16 v[128:143], v[32:35], v[64:71], v3\n"
    "v_exp_f32 v79, v79\n"
    "v_exp_f32 v80, v80\n"
    "v_exp_f32 v81, v81\n"
    "v_smfmac_f32_32x32x32_bf16 v[128:143], v[36:39], v[72:79], v3\n"
    "s_waitcnt lgkmcnt(0)\n"
    "v_readfirstlane_b32 s59, v13\n"
    "s_cmp_eq_u32 s59, 4\n"
    "s_cbranch_scc0 .LA_opbusy\n"
    ".LA_opfree:\n"
    "v_exp_f32 v82, v82\n"
    "v_exp_f32 v83, v83\n"
    "v_exp_f32 v84, v84\n"
    "v_mfma_f32_32x32x16_bf16 v[64:79], v[28:31], v[104:107], 0\n"
    "v_exp_f32 v85, v85\n"
    "v_exp_f32 v86, v86\n"
    "v_exp_f32 v87, v87\n"
    "v_exp_f32 v88, v88\n"
    "v_exp_f32 v89, v89\n"
    "v_exp_f32 v90, v90\n"
    "v_exp_f32 v91, v91\n"
    "v_exp_f32 v92, v92\n"
    "v_exp_f32 v93, v93\n"
    "v_exp_f32 v94, v94\n"
    "v_smfmac_f32_32x32x32_bf16 v[144:159], v[32:35], v[80:87], v3\n"
    "v_exp_f32 v95, v95\n"
    "v_exp_f32 v64, v64\n"
    "v_exp_f32 v65, v65\n"
    "v_smfmac_f32_32x32x32_bf16 v[144:159], v[36:39], v[88:95], v3\n"
    "ds_write_b128 v26, v[128:131] offset:0\n"
    "v_exp_f32 v66, v66\n"
    "v_exp_f32 v67, v67\n"
    "v_exp_f32 v68, v68\n"
    "v_mfma_f32_32x32x16_bf16 v[80:95], v[28:31], v[108:111], 0\n"
    "ds_write_b128 v26, v[132:135] offset:1024\n"
    "v_exp_f32 v69, v69\n"
    "v_exp_f32 v70, v70\n"
    "v_exp_f32 v71, v71\n"
    "v_exp_f32 v72, v72\n"
    "v_exp_f32 v73, v73\n"
    "v_exp_f32 v74, v74\n"
    "v_exp_f32 v75, v75\n"
    "v_exp_f32 v76, v76\n"
    "v_exp_f32 v77, v77\n"
    "v_exp_f32 v78, v78\n"
    "v_smfmac_f32_32x32x32_bf16 v[160:175], v[32:35], v[64:71], v3\n"
    "ds_write_b32 v27, v136 offset:2048\n"
    "v_exp_f32 v79, v79\n"
    "v_exp_f32 v80, v80\n"
    "v_exp_f32 v81, v81\n"
    "v_smfmac_f32_32x32x32_bf16 v[160:175], v[36:39], v[72:79], v3\n"
    "ds_write_b128 v26, v[144:147] offset:2304\n"
    "v_exp_f32 v82, v82\n"
    "v_exp_f32 v83, v83\n"
    "v_exp_f32 v84, v84\n"
    "v_mfma_f32_32x32x16_bf16 v[64:79], v[28:31], v[112:115], 0\n"
    "ds_write_b128 v26, v[148:151] offset:3328\n"
    "v_exp_f32 v85, v85\n"
    "v_exp_f32 v86, v86\n"
    "v_exp_f32 v87, v87\n"
    "v_exp_f32 v88, v88\n"
    "v_exp_f32 v89, v89\n"
    "v_exp_f32 v90, v90\n"
    "v_exp_f32 v91, v91\n"
    "v_exp_f32 v92, v92\n"
    "v_exp_f32 v93, v93\n"
    "v_exp_f32 v94, v94\n"
    "v_smfmac_f32_32x32x32_bf16 v[176:191], v[32:35], v[80:87], v3\n"
    "ds_write_b32 v27, v152 offset:4352\n"
    "v_exp_f32 v95, v95\n"
    "v_exp_f32 v64, v64\n"
    "v_exp_f32 v65, v65\n"
    "v_smfmac_f32_32x32x32_bf16 v[176:191], v[36:39], v[88:95], v3\n"
    "ds_write_b128 v26, v[160:163] offset:4608\n"
    "v_exp_f32 v66, v66\n"
    "v_exp_f32 v67, v67\n"
    "v_exp_f32 v68, v68\n"
    "v_mfma_f32_32x32x16_bf16 v[80:95], v[28:31], v[116:119], 0\n"
    "ds_write_b128 v26, v[164:167] offset:5632\n"
    "v_exp_f32 v69, v69\n"
    "v_exp_f32 v70, v70\n"
    "v_exp_f32 v71, v71\n"
    "v_exp_f32 v72, v72\n"
    "v_exp_f32 v73, v73\n"
    "v_exp_f32 v74, v74\n"
    "v_exp_f32 v75, v75\n"
    "v_exp_f32 v76, v76\n"
    "v_exp_f32 v77, v77\n"
    "v_exp_f32 v78, v78\n"
    "v_smfmac_f32_32x32x32_bf16 v[192:207], v[32:35], v[64:71], v3\n"
    "ds_write_b32 v27, v168 offset:6656\n"
    "v_exp_f32 v79, v79\n"
    "v_exp_f32 v80, v80\n"
    "v_exp_f32 v81, v81\n"
    "v_smfmac_f32_32x32x32_bf16 v[192:207], v[36:39], v[72:79], v3\n"
    "ds_write_b128 v26, v[176:179] offset:6912\n"
    "v_exp_f32 v82, v82\n"
    "v_exp_f32 v83, v83\n"
    "v_exp_f32 v84, v84\n"
    "v_mfma_f32_32x32x16_bf16 v[64:79], v[28:31], v[120:123], 0\n"
    "ds_write_b128 v26, v[180:183] offset:7936\n"
    "v_exp_f32 v85, v85\n"
    "v_exp_f32 v86, v86\n"
    "v_exp_f32 v87, v87\n"
    "v_exp_f32 v88, v88\n"
    "v_exp_f32 v89, v89\n"
    "v_exp_f32 v90, v90\n"
    "v_exp_f32 v91, v91\n"
    "v_exp_f32 v92, v92\n"
    "v_exp_f32 v93, v93\n"
    "v_exp_f32 v94, v94\n"
    "v_smfmac_f32_32x32x32_bf16 v[208:223], v[32:35], v[80:87], v3\n"
    "ds_write_b32 v27, v184 offset:8960\n"
    "v_exp_f32 v95, v95\n"
    "v_exp_f32 v64, v64\n"
    "v_exp_f32 v65, v65\n"
    "v_smfmac_f32_32x32x32_bf16 v[208:223], v[36:39], v[88:95], v3\n"
    "ds_write_b128 v26, v[192:195] offset:9216\n"
    "v_exp_f32 v66, v66\n"
    "v_exp_f32 v67, v67\n"
    "v_exp_f32 v68, v68\n"
    "v_mfma_f32_32x32x16_bf16 v[80:95], v[28:31], v[124:127], 0\n"
    "ds_write_b128 v26, v[196:199] offset:10240\n"
    "v_exp_f32 v69, v69\n"
    "v_exp_f32 v70, v70\n"
    "v_exp_f32 v71, v71\n"
    "v_exp_f32 v72, v72\n"
    "v_exp_f32 v73, v73\n"
    "v_exp_f32 v74, v74\n"
    "v_exp_f32 v75, v75\n"
    "v_exp_f32 v76, v76\n"
    "v_exp_f32 v77, v77\n"
    "v_exp_f32 v78, v78\n"
    "v_smfmac_f32_32x32x32_bf16 v[224:239], v[32:35], v[64:71], v3\n"
    "ds_write_b32 v27, v200 offset:11264\n"
    "v_exp_f32 v79, v79\n"
    "v_exp_f32 v80, v80\n"
    "v_exp_f32 v81, v81\n"
    "v_smfmac_f32_32x32x32_bf16 v[224:239], v[36:39], v[72:79], v3\n"
    "ds_write_b128 v26, v[208:211] offset:11520\n"
    "ds_write_b32 v27, v216 offset:13568\n"
    "v_exp_f32 v82, v82\n"
    "v_exp_f32 v83, v83\n"
    "v_exp_f32 v84, v84\n"
    "ds_write_b128 v26, v[212:215] offset:12544\n"
    "v_exp_f32 v85, v85\n"
    "v_exp_f32 v86, v86\n"
    "v_exp_f32 v87, v87\n"
    "v_exp_f32 v88, v88\n"
    "v_exp_f32 v89, v89\n"
    "v_smfmac_f32_32x32x32_bf16 v[240:255], v[32:35], v[80:87], v3\n"
    "s_waitcnt lgkmcnt(0)\n"
    "s_mov_b64 exec, 1\n"
    "ds_add_u32 v14, v15 offset:4\n"
    "s_mov_b64 exec, -1\n"
    "v_exp_f32 v90, v90\n"
    "v_exp_f32 v91, v91\n"
    "v_exp_f32 v92, v92\n"
    "v_exp_f32 v93, v93\n"
    "v_exp_f32 v94, v94\n"
    "v_exp_f32 v95, v95\n"
    "ds_read_b32 v13, v14 offset:4\n"
    "s_nop 0\n"
    "v_smfmac_f32_32x32x32_bf16 v[240:255], v[36:39], v[88:95], v3\n"
    "s_waitcnt lgkmcnt(0)\n"
    "v_readfirstlane_b32 s59, v13\n"
    "s_cmp_eq_u32 s59, 4\n"
    "s_cbranch_scc1 .LA_opw\n"
    "s_mov_b32 s67, 400000\n"
    ".Lpoll0:\n"
    "ds_read_b32 v13, v14 offset:4\n"
    "s_waitcnt lgkmcnt(0)\n"
    "v_readfirstlane_b32 s59, v13\n"
    "s_cmp_eq_u32 s59, 4\n"
    "s_cbranch_scc1 .Lpolld0\n"
    "s_sub_u32 s67, s67, 1\n"
    "s_cbranch_scc0 .Lpoll0\n"
    ".Lpolld0:\n"
    ".LA_opw:\n"
    "s_setprio 3\n"
    "s_waitcnt vmcnt(0)\n"
    "ds_read_b128 v[128:131], v20 offset:0\n"
    "ds_read_b128 v[132:135], v20 offset:1024\n"
    "ds_read_b32 v136, v21 offset:2048\n"
    "ds_read_b128 v[160:163], v22 offset:4608\n"
    "ds_read_b128 v[164:167], v22 offset:5632\n"
    "ds_read_b32 v168, v23 offset:6656\n"
    "ds_read_b128 v[192:195], v24 offset:9216\n"
    "ds_read_b128 v[196:199], v24 offset:10240\n"
    "ds_read_b32 v200, v25 offset:11264\n"
    "ds_read_b128 v[144:147], v20 offset:2304\n"
    "ds_read_b128 v[148:151], v20 offset:3328\n"
    "ds_read_b32 v152, v21 offset:4352\n"
    "ds_read_b128 v[176:179], v22 offset:6912\n"
    "ds_read_b128 v[180:183], v22 offset:7936\n"
    "ds_read_b32 v184, v23 offset:8960\n"
    "ds_read_b128 v[208:211], v24 offset:11520\n"
    "ds_read_b128 v[212:215], v24 offset:12544\n"
    "ds_read_b32 v216, v25 offset:13568\n"
    "s_waitcnt lgkmcnt(15)\n"
    "v_add_f32 v232, v232, v136\n"
    "v_add_f32 v224, v224, v128\n"
    "v_add_f32 v225, v225, v129\n"
    "v_add_f32 v226, v226, v130\n"
    "v_add_f32 v227, v227, v131\n"
    "v_add_f32 v228, v228, v132\n"
    "v_add_f32 v229, v229, v133\n"
    "v_add_f32 v230, v230, v134\n"
    "v_add_f32 v231, v231, v135\n"
    "s_waitcnt lgkmcnt(12)\n"
    "v_add_f32 v232, v232, v168\n"
    "v_add_f32 v224, v224, v160\n"
    "v_add_f32 v225, v225, v161\n"
    "v_add_f32 v226, v226, v162\n"
    "v_add_f32 v227, v227, v163\n"
    "v_add_f32 v228, v228, v164\n"
    "v_add_f32 v229, v229, v165\n"
    "v_add_f32 v230, v230, v166\n"
    "v_add_f32 v231, v231, v167\n"
    "s_waitcnt lgkmcnt(9)\n"
    "v_add_f32 v232, v232, v200\n"
    "v_add_f32 v224, v224, v192\n"
    "v_add_f32 v225, v225, v193\n"
    "v_add_f32 v226, v226, v194\n"
    "v_add_f32 v227, v227, v195\n"
    "v_add_f32 v228, v228, v196\n"
    "v_add_f32 v229, v229, v197\n"
    "v_add_f32 v230, v230, v198\n"
    "v_add_f32 v231, v231, v199\n"
    "s_waitcnt lgkmcnt(6)\n"
    "v_add_f32 v248, v248, v152\n"
    "v_add_f32 v240, v240, v144\n"
    "v_add_f32 v241, v241, v145\n"
    "v_add_f32 v242, v242, v146\n"
    "v_add_f32 v243, v243, v147\n"
    "v_add_f32 v244, v244, v148\n"
    "v_add_f32 v245, v245, v149\n"
    "v_add_f32 v246, v246, v150\n"
    "v_add_f32 v247, v247, v151\n"
    "s_waitcnt lgkmcnt(3)\n"
    "v_add_f32 v248, v248, v184\n"
    "v_add_f32 v240, v240, v176\n"
    "v_add_f32 v241, v241, v177\n"
    "v_add_f32 v242, v242, v178\n"
    "v_add_f32 v243, v243, v179\n"
    "v_add_f32 v244, v244, v180\n"
    "v_add_f32 v245, v245, v181\n"
    "v_add_f32 v246, v246, v182\n"
    "v_add_f32 v247, v247, v183\n"
    "s_waitcnt lgkmcnt(0)\n"
    "v_add_f32 v248, v248, v216\n"
    "v_add_f32 v240, v240, v208\n"
    "v_add_f32 v241, v241, v209\n"
    "v_add_f32 v242, v242, v210\n"
    "v_add_f32 v243, v243, v211\n"
    "v_add_f32 v244, v244, v212\n"
    "v_add_f32 v245, v245, v213\n"
    "v_add_f32 v246, v246, v214\n"
    "v_add_f32 v247, v247, v215\n"
    "s_mov_b64 exec, 1\n"
    "ds_add_u32 v14, v15 offset:12\n"
    "s_mov_b64 exec, -1\n"
    "v_maximum3_f32 v12, abs(v224), abs(v225), abs(v226)\n"
    "v_maximum3_f32 v12, v12, abs(v227), abs(v228)\n"
    "v_maximum3_f32 v12, v12, abs(v229), abs(v230)\n"
    "v_maximum3_f32 v12, v12, abs(v231), abs(v240)\n"
    "v_maximum3_f32 v12, v12, abs(v241), abs(v242)\n"
    "v_maximum3_f32 v12, v12, abs(v243), abs(v244)\n"
    "v_maximum3_f32 v12, v12, abs(v245), abs(v246)\n"
    "v_maximum3_f32 v12, v12, abs(v247), abs(v247)\n"
    "v_mul_f32 v12, 0x2f800000, v12\n"
    "v_maximum3_f32 v12, v12, v232, v248\n"
    "v_min_f32 v13, v232, v248\n"
    "v_cmp_nge_f32 vcc, 0x6c800000, v12\n"
    "s_mov_b64 s[60:61], vcc\n"
    "v_cmp_nle_f32 vcc, 0x2b800000, v13\n"
    "v_rcp_f32 v234, v232\n"
    "v_rcp_f32 v250, v248\n"
    "s_or_b64 vcc, vcc, s[60:61]\n"
    "s_cbranch_scc1 .LA_fb\n"
    ".LA_fbret:\n"
    "v_mul_f32 v234, v11, v234\n"
    "v_mul_f32 v250, v11, v250\n"
    "v_mul_f32 v224, v234, v224\n"
    "v_mul_f32 v225, v234, v225\n"
    "v_mul_f32 v226, v234, v226\n"
    "v_mul_f32 v227, v234, v227\n"
    "v_mul_f32 v228, v234, v228\n"
    "v_mul_f32 v229, v234, v229\n"
    "v_mul_f32 v230, v234, v230\n"
    "v_mul_f32 v231, v234, v231\n"
    "v_cvt_pk_bf16_f32 v236, v224, v225\n"
    "v_cvt_pk_bf16_f32 v237, v226, v227\n"
    "v_cvt_pk_bf16_f32 v238, v228, v229\n"
    "v_cvt_pk_bf16_f32 v239, v230, v231\n"
    "buffer_store_dwordx4 v[236:239], v10, s[28:31], s90 offen offset:0 sc1\n"
    "v_mul_f32 v240, v250, v240\n"
    "v_mul_f32 v241, v250, v241\n"
    "v_mul_f32 v242, v250, v242\n"
    "v_mul_f32 v243, v250, v243\n"
    "v_mul_f32 v244, v250, v244\n"
    "v_mul_f32 v245, v250, v245\n"
    "v_mul_f32 v246, v250, v246\n"
    "v_mul_f32 v247, v250, v247\n"
    "v_cvt_pk_bf16_f32 v252, v240, v241\n"
    "v_cvt_pk_bf16_f32 v253, v242, v243\n"
    "v_cvt_pk_bf16_f32 v254, v244, v245\n"
    "v_cvt_pk_bf16_f32 v255, v246, v247\n"
    "buffer_store_dwordx4 v[252:255], v10, s[28:31], s90 offen offset:1024 sc1\n"
    "v_mov_b32 v16, s56\n"
    "v_mov_b32 v17, s57\n"
    "v_lshlrev_b32 v18, 3, v1\n"
    "s_mov_b64 exec, 0xffff\n"
    "buffer_store_dwordx2 v[16:17], v18, s[28:31], s91 offen sc1\n"
    "s_mov_b64 exec, -1\n"
    "s_endpgm\n"
    ".LA_kvwait:\n"
    "s_sub_u32 s67, s67, 1\n"
    "s_cbranch_scc1 .LA_kvok\n"
    "s_sleep 1\n"
    "buffer_load_dwordx2 v[62:63], v13, s[28:31], s62 offen sc1\n"
    "s_branch .LA_kvchk\n"
    ".LA_kvrest:\n"
    "s_mov_b32 s67, 100000\n"
    ".Lpoll1:\n"
    "buffer_load_dwordx2 v[4:5], v13, s[28:31], s62 offen sc1\n"
    "s_waitcnt vmcnt(0)\n"
    "v_cmp_ne_u64 vcc, s[56:57], v[4:5]\n"
    "s_nop 1\n"
    "s_cmp_eq_u64 vcc, 0\n"
    "s_cbranch_scc1 .Lpolld1\n"
    "s_sleep 1\n"
    "s_sub_u32 s67, s67, 1\n"
    "s_cbranch_scc0 .Lpoll1\n"
    ".Lpolld1:\n"
    "s_mov_b32 s64, 0\n"
    "s_branch .LA_kvall\n"
    ".LA_grwait:\n"
    "s_sub_u32 s67, s67, 1\n"
    "s_cbranch_scc1 .LA_grok\n"
    "buffer_load_dwordx4 v[64:67], v10, s[28:31], s86 offen offset:0 sc1\n"
    "buffer_load_dwordx4 v[68:71], v10, s[28:31], s86 offen offset:1024 sc1\n"
    "buffer_load_dwordx4 v[72:75], v4, s[28:31], s86 offen offset:2048 sc1\n"
    "buffer_load_dwordx4 v[76:79], v4, s[28:31], s86 offen offset:3072 sc1\n"
    "buffer_load_dwordx4 v[80:83], v4, s[28:31], s86 offen offset:2560 sc1\n"
    "buffer_load_dwordx4 v[84:87], v4, s[28:31], s86 offen offset:3584 sc1\n"
    "s_waitcnt vmcnt(0)\n"
    "s_branch .LA_grchk\n"
    ".LA_opbusy:\n"
    "s_mov_b32 s67, 400000\n"
    ".Lpoll2:\n"
    "ds_read_b32 v13, v14 offset:8\n"
    "s_waitcnt lgkmcnt(0)\n"
    "v_readfirstlane_b32 s59, v13\n"
    "s_cmp_eq_u32 s59, 4\n"
    "s_cbranch_scc1 .Lpolld2\n"
    "s_sleep 1\n"
    "s_sub_u32 s67, s67, 1\n"
    "s_cbranch_scc0 .Lpoll2\n"
    ".Lpolld2:\n"
    "s_branch .LA_opfree\n"
    ".LA_fb:\n"
    "v_mov_b32 v16, 0xff800000\n"
    "v_mov_b32 v17, 0xff800000\n"
    "s_mov_b32 s92, 65536\n"
    "s_mov_b32 s63, 64\n"
    ".LA_fb1:\n"
    "buffer_load_dwordx4 v[28:31], v10, s[28:31], s92 offen sc1\n"
    "buffer_load_dwordx4 v[32:35], v2, s[28:31], s92 offen offset:1024 sc1\n"
    "buffer_load_dwordx4 v[36:39], v2, s[28:31], s92 offen offset:1664 sc1\n"
    "s_add_u32 s92, s92, 2304\n"
    "s_waitcnt vmcnt(0)\n"
    "v_mfma_f32_32x32x16_bf16 v[64:79], v[28:31], v[120:123], 0\n"
    "s_nop 12\n"
    "v_max3_f32 v16, v16, v64, v65\n"
    "v_max3_f32 v16, v16, v66, v67\n"
    "v_max3_f32 v16, v16, v68, v69\n"
    "v_max3_f32 v16, v16, v70, v71\n"
    "v_max3_f32 v16, v16, v72, v73\n"
    "v_max3_f32 v16, v16, v74, v75\n"
    "v_max3_f32 v16, v16, v76, v77\n"
    "v_max3_f32 v16, v16, v78, v79\n"
    "v_mfma_f32_32x32x16_bf16 v[64:79], v[28:31], v[124:127], 0\n"
    "s_nop 12\n"
    "v_max3_f32 v17, v17, v64, v65\n"
    "v_max3_f32 v17, v17, v66, v67\n"
    "v_max3_f32 v17, v17, v68, v69\n"
    "v_max3_f32 v17, v17, v70, v71\n"
    "v_max3_f32 v17, v17, v72, v73\n"
    "v_max3_f32 v17, v17, v74, v75\n"
    "v_max3_f32 v17, v17, v76, v77\n"
    "v_max3_f32 v17, v17, v78, v79\n"
    "s_sub_u32 s63, s63, 1\n"
    "s_cmp_lg_u32 s63, 0\n"
    "s_cbranch_scc1 .LA_fb1\n"
    "v_mov_b32 v18, v16\n"
    "s_nop 1\n"
    "v_permlane32_swap_b32 v16, v18\n"
    "s_nop 1\n"
    "v_max_f32 v16, v16, v18\n"
    "v_mov_b32 v18, v17\n"
    "s_nop 1\n"
    "v_permlane32_swap_b32 v17, v18\n"
    "s_nop 1\n"
    "v_max_f32 v17, v17, v18\n"
    "v_mov_b32 v224, 0\n"
    "v_mov_b32 v225, 0\n"
    "v_mov_b32 v226, 0\n"
    "v_mov_b32 v227, 0\n"
    "v_mov_b32 v228, 0\n"
    "v_mov_b32 v229, 0\n"
    "v_mov_b32 v230, 0\n"
    "v_mov_b32 v231, 0\n"
    "v_mov_b32 v232, 0\n"
    "v_mov_b32 v233, 0\n"
    "v_mov_b32 v234, 0\n"
    "v_mov_b32 v235, 0\n"
    "v_mov_b32 v236, 0\n"
    "v_mov_b32 v237, 0\n"
    "v_mov_b32 v238, 0\n"
    "v_mov_b32 v239, 0\n"
    "v_mov_b32 v240, 0\n"
    "v_mov_b32 v241, 0\n"
    "v_mov_b32 v242, 0\n"
    "v_mov_b32 v243, 0\n"
    "v_mov_b32 v244, 0\n"
    "v_mov_b32 v245, 0\n"
    "v_mov_b32 v246, 0\n"
    "v_mov_b32 v247, 0\n"
    "v_mov_b32 v248, 0\n"
    "v_mov_b32 v249, 0\n"
    "v_mov_b32 v250, 0\n"
    "v_mov_b32 v251, 0\n"
    "v_mov_b32 v252, 0\n"
    "v_mov_b32 v253, 0\n"
    "v_mov_b32 v254, 0\n"
    "v_mov_b32 v255, 0\n"
    "s_mov_b32 s92, 65536\n"
    "s_mov_b32 s63, 64\n"
    ".LA_fb2:\n"
    "buffer_load_dwordx4 v[28:31], v10, s[28:31], s92 offen sc1\n"
    "buffer_load_dwordx4 v[32:35], v2, s[28:31], s92 offen offset:1024 sc1\n"
    "buffer_load_dwordx4 v[36:39], v2, s[28:31], s92 offen offset:1664 sc1\n"
    "s_add_u32 s92, s92, 2304\n"
    "s_waitcnt vmcnt(0)\n"
    "v_mfma_f32_32x32x16_bf16 v[64:79], v[28:31], v[120:123], 0\n"
    "s_nop 12\n"
    "v_sub_f32 v64, v64, v16\n"
    "v_sub_f32 v65, v65, v16\n"
    "v_sub_f32 v66, v66, v16\n"
    "v_sub_f32 v67, v67, v16\n"
    "v_sub_f32 v68, v68, v16\n"
    "v_sub_f32 v69, v69, v16\n"
    "v_sub_f32 v70, v70, v16\n"
    "v_sub_f32 v71, v71, v16\n"
    "v_sub_f32 v72, v72, v16\n"
    "v_sub_f32 v73, v73, v16\n"
    "v_sub_f32 v74, v74, v16\n"
    "v_sub_f32 v75, v75, v16\n"
    "v_sub_f32 v76, v76, v16\n"
    "v_sub_f32 v77, v77, v16\n"
    "v_sub_f32 v78, v78, v16\n"
    "v_sub_f32 v79, v79, v16\n"
    "v_exp_f32 v64, v64\n"
    "v_exp_f32 v65, v65\n"
    "v_exp_f32 v66, v66\n"
    "v_exp_f32 v67, v67\n"
    "v_exp_f32 v68, v68\n"
    "v_exp_f32 v69, v69\n"
    "v_exp_f32 v70, v70\n"
    "v_exp_f32 v71, v71\n"
    "v_exp_f32 v72, v72\n"
    "v_exp_f32 v73, v73\n"
    "v_exp_f32 v74, v74\n"
    "v_exp_f32 v75, v75\n"
    "v_exp_f32 v76, v76\n"
    "v_exp_f32 v77, v77\n"
    "v_exp_f32 v78, v78\n"
    "v_exp_f32 v79, v79\n"
    "s_nop 1\n"
    "v_smfmac_f32_32x32x32_bf16 v[224:239], v[32:35], v[64:71], v3\n"
    "v_smfmac_f32_32x32x32_bf16 v[224:239], v[36:39], v[72:79], v3\n"
    "v_mfma_f32_32x32x16_bf16 v[64:79], v[28:31], v[124:127], 0\n"
    "s_nop 12\n"
    "v_sub_f32 v64, v64, v17\n"
    "v_sub_f32 v65, v65, v17\n"
    "v_sub_f32 v66, v66, v17\n"
    "v_sub_f32 v67, v67, v17\n"
    "v_sub_f32 v68, v68, v17\n"
    "v_sub_f32 v69, v69, v17\n"
    "v_sub_f32 v70, v70, v17\n"
    "v_sub_f32 v71, v71, v17\n"
    "v_sub_f32 v72, v72, v17\n"
    "v_sub_f32 v73, v73, v17\n"
    "v_sub_f32 v74, v74, v17\n"
    "v_sub_f32 v75, v75, v17\n"
    "v_sub_f32 v76, v76, v17\n"
    "v_sub_f32 v77, v77, v17\n"
    "v_sub_f32 v78, v78, v17\n"
    "v_sub_f32 v79, v79, v17\n"
    "v_exp_f32 v64, v64\n"
    "v_exp_f32 v65, v65\n"
    "v_exp_f32 v66, v66\n"
    "v_exp_f32 v67, v67\n"
    "v_exp_f32 v68, v68\n"
    "v_exp_f32 v69, v69\n"
    "v_exp_f32 v70, v70\n"
    "v_exp_f32 v71, v71\n"
    "v_exp_f32 v72, v72\n"
    "v_exp_f32 v73, v73\n"
    "v_exp_f32 v74, v74\n"
    "v_exp_f32 v75, v75\n"
    "v_exp_f32 v76, v76\n"
    "v_exp_f32 v77, v77\n"
    "v_exp_f32 v78, v78\n"
    "v_exp_f32 v79, v79\n"
    "s_nop 1\n"
    "v_smfmac_f32_32x32x32_bf16 v[240:255], v[32:35], v[64:71], v3\n"
    "v_smfmac_f32_32x32x32_bf16 v[240:255], v[36:39], v[72:79], v3\n"
    "s_sub_u32 s63, s63, 1\n"
    "s_cmp_lg_u32 s63, 0\n"
    "s_cbranch_scc1 .LA_fb2\n"
    "s_nop 12\n"
    "v_rcp_f32 v234, v232\n"
    "v_rcp_f32 v250, v248\n"
    "s_nop 0\n"
    "s_branch .LA_fbret\n"
    ".Lend:\n"
    : "+{v0}"(tid)
    : "{s[0:1]}"(__builtin_amdgcn_kernarg_segment_ptr()), "{s2}"(blockIdx.x), "{s3}"(ldsb)
    : "v1", "v2", "v3", "v4", "v5", "v6", "v7", "v8", "v9", "v10", "v11", "v12", "v13", "v14", "v15", "v16", "v17", "v18", "v19", "v20", "v21", "v22", "v23", "v24", "v25", "v26", "v27", "v28", "v29", "v30", "v31", "v32", "v33", "v34", "v35", "v36", "v37", "v38", "v39", "v40", "v41", "v42", "v43", "v44", "v45", "v46", "v47", "v48", "v49", "v50", "v51", "v52", "v53", "v54", "v55", "v56", "v57", "v58", "v59", "v60", "v61", "v62", "v63", "v64", "v65", "v66", "v67", "v68", "v69", "v70", "v71", "v72", "v73", "v74", "v75", "v76", "v77", "v78", "v79", "v80", "v81", "v82", "v83", "v84", "v85", "v86", "v87", "v88", "v89", "v90", "v91", "v92", "v93", "v94", "v95", "v96", "v97", "v98", "v99", "v100", "v101", "v102", "v103", "v104", "v105", "v106", "v107", "v108", "v109", "v110", "v111", "v112", "v113", "v114", "v115", "v116", "v117", "v118", "v119", "v120", "v121", "v122", "v123", "v124", "v125", "v126", "v127", "v128", "v129", "v130", "v131", "v132", "v133", "v134", "v135", "v136", "v137", "v138", "v139", "v140", "v141", "v142", "v143", "v144", "v145", "v146", "v147", "v148", "v149", "v150", "v151", "v152", "v153", "v154", "v155", "v156", "v157", "v158", "v159", "v160", "v161", "v162", "v163", "v164", "v165", "v166", "v167", "v168", "v169", "v170", "v171", "v172", "v173", "v174", "v175", "v176", "v177", "v178", "v179", "v180", "v181", "v182", "v183", "v184", "v185", "v186", "v187", "v188", "v189", "v190", "v191", "v192", "v193", "v194", "v195", "v196", "v197", "v198", "v199", "v200", "v201", "v202", "v203", "v204", "v205", "v206", "v207", "v208", "v209", "v210", "v211", "v212", "v213", "v214", "v215", "v216", "v217", "v218", "v219", "v220", "v221", "v222", "v223", "v224", "v225", "v226", "v227", "v228", "v229", "v230", "v231", "v232", "v233", "v234", "v235", "v236", "v237", "v238", "v239", "v240", "v241", "v242", "v243", "v244", "v245", "v246", "v247", "v248", "v249", "v250", "v251", "v252", "v253", "v254", "v255", "s4", "s5", "s6", "s7", "s8", "s9", "s10", "s11", "s12", "s13", "s14", "s15", "s16", "s17", "s18", "s19", "s20", "s21", "s22", "s23", "s24", "s25", "s26", "s27", "s28", "s29", "s30", "s31", "s33", "s34", "s35", "s36", "s37", "s38", "s39", "s40", "s41", "s42", "s43", "s44", "s45", "s46", "s47", "s48", "s49", "s50", "s51", "s52", "s53", "s54", "s55", "s56", "s57", "s58", "s59", "s60", "s61", "s62", "s63", "s64", "s65", "s66", "s67", "s68", "s69", "s70", "s71", "s72", "s73", "s74", "s75", "s76", "s77", "s78", "s79", "s80", "s81", "s82", "s83", "s84", "s85", "s86", "s87", "s88", "s89", "s90", "s91", "s92", "s93", "s94", "s95", "s96", "s97", "s98", "s99", "vcc", "scc", "memory");
}

extern "C" void kernel_launch(void* const* d_in, const int* in_sizes, int n_in, void* d_out, int out_size, void* d_ws, size_t ws_size,
                              hipStream_t stream) {
  fused_block_kernel<<<256, 512, 0, stream>>>(d_in[0], d_in[1], d_in[2], d_in[3], d_in[4], d_in[6], d_in[7], d_in[8], d_out, d_ws);
}
```
